```python
import torch

def _softmax(x, axis=-1):
    return torch.softmax(x, dim=axis)
def _gelu(x):
    return torch.nn.functional.gelu(x, approximate="tanh")
def _silu(x):
    return torch.nn.functional.silu(x)
def _sigmoid(x):
    return torch.sigmoid(x)
def _layernorm(x, axis=-1, eps=1e-5):
    m = x.mean(dim=axis, keepdim=True)
    v = x.var(dim=axis, keepdim=True, unbiased=False)
    return (x - m) / torch.sqrt(v + eps)
def _scatter_add(tgt, idx, src, axis=0):
    return tgt.clone().index_add_(axis, idx, src)
def _segment_sum(src, seg, n):
    out = torch.zeros((n,) + src.shape[1:], dtype=src.dtype)
    return out.index_add_(0, seg, src)
def _pad(x, pw):
    flat = tuple(v for pair in reversed(pw) for v in pair)
    return torch.nn.functional.pad(x, flat)

kernel_name = 'recurrent_scan__mha_block_cached__geglu_ffn__decode'

def setup_inputs():

    _w = (lambda a: (lambda t: t.to(torch.bfloat16) if t.is_floating_point() else t)(torch.as_tensor(a)))
    def _A_setup():
        B, Sq, D = 2, 1, 512
        H, Sk, Dh = 32, 16384, 128
        return {
            "x": _w((lambda *s: torch.randn(*s, dtype=torch.bfloat16) * 0.05)(B, Sq, D)),
            "k_cache": _w((lambda *s: torch.randn(*s, dtype=torch.bfloat16) * 1.6)(B, H, Sk, Dh)),
            "v_cache": _w((lambda *s: torch.randn(*s, dtype=torch.bfloat16) * 0.2)(B, H, Sk, Dh)),
            "wq": _w((lambda *s: torch.randn(*s, dtype=torch.bfloat16) * 1.6)(D, H * Dh) * (D ** -0.5)),
            "wo": _w((lambda *s: torch.randn(*s, dtype=torch.bfloat16) * 0.2)(H * Dh, D) * ((H * Dh) ** -0.5)),
        }
    _A_in = _A_setup()

    def _B_setup():
        B, S, D, F = 2, 1, 512, 1376
        return {
            "x": _w((lambda *s: torch.randn(*s, dtype=torch.bfloat16) * 0.05)(B, S, D)),
            "w_gate": _w((lambda *s: torch.randn(*s, dtype=torch.bfloat16) * 0.1)(D, F)),
            "w_up": _w((lambda *s: torch.randn(*s, dtype=torch.bfloat16) * 0.1)(D, F)),
            "w_down": _w((lambda *s: torch.randn(*s, dtype=torch.bfloat16) * 0.1)(F, D)),
        }
    _B_in = _B_setup()

    return {
        "A_x": _A_in["x"],
        "A_k_cache": _A_in["k_cache"],
        "A_v_cache": _A_in["v_cache"],
        "A_wq": _A_in["wq"],
        "A_wo": _A_in["wo"],
        "B_x": _B_in["x"],
        "B_w_gate": _B_in["w_gate"],
        "B_w_up": _B_in["w_up"],
        "B_w_down": _B_in["w_down"],
    }

def reference(A_x, A_k_cache, A_v_cache, A_wq, A_wo, B_x, B_w_gate, B_w_up, B_w_down):
    def _A_ref(x, k_cache, v_cache, wq, wo):
        B, Sq, D = x.shape
        H, Dh = 32, 128
        q = (lambda x, *d: x.permute(*d))((x @ wq).reshape(B, Sq, H, Dh), 0, 2, 1, 3)
        scores = torch.einsum("bhqd,bhkd->bhqk", q, k_cache) * (Dh ** -0.5)
        p = _softmax(scores, axis=-1)
        o = torch.einsum("bhqk,bhkd->bhqd", p, v_cache)
        return (lambda x, *d: x.permute(*d))(o, 0, 2, 1, 3).reshape(B, Sq, H * Dh) @ wo

    def _B_ref(x, w_gate, w_up, w_down):
        return (_gelu(x @ w_gate) * (x @ w_up)) @ w_down

    x = A_x
    xn = _layernorm(x, axis=-1)
    s = _A_ref(xn, A_k_cache, A_v_cache, A_wq, A_wo)
    g = _silu(_B_ref(xn, B_w_gate, B_w_up, B_w_down))
    return x + s * g
```

```cpp
#include <hip/hip_runtime.h>
#include <stdint.h>

typedef unsigned u32;
typedef unsigned long long u64;
typedef u32 u32x4 __attribute__((ext_vector_type(4)));
typedef float f32x4 __attribute__((ext_vector_type(4)));
typedef float f32x2 __attribute__((ext_vector_type(2)));
typedef __bf16 bf16x2 __attribute__((ext_vector_type(2)));
#define GAS __attribute__((address_space(1)))
typedef GAS u64 gu64;
typedef GAS u32 gu32;
#define RLX_AGENT __ATOMIC_RELAXED, __HIP_MEMORY_SCOPE_AGENT

constexpr int DM = 512, NH = 32, SK = 16384, DH = 128, FF = 1376;
constexpr int NW = 8, NTHR = NW * 64, NWG = 256;
constexpr int T = 4;
constexpr int UKEYS = 4 * T;
constexpr int UNITS = SK / UKEYS;
constexpr int NSLICE = 43, FS = 32;
constexpr int NRED = 32, RC = 16;
constexpr int FFN_OFF = 12;
constexpr int RED_OFF = 6;
constexpr u32 SPIN_CAP = 1u << 20;

constexpr size_t WS_EPOCH = 0;
constexpr size_t WS_ML = 4096;
constexpr size_t WS_T = 8192;
constexpr size_t WS_F = WS_T + (size_t)NWG * DM * 8;
constexpr size_t WS_END = WS_F + (size_t)NSLICE * 2 * DM * 8;

constexpr int L_XN = 0;
constexpr int L_QP = L_XN + 2 * DM;
constexpr int L_QB = L_QP + NW * DH;
constexpr int L_MM = L_QB + 64;
constexpr int L_LL = L_MM + 16;
constexpr int L_HS = L_LL + 16;
constexpr int L_G = L_HS + 128;
constexpr int L_BIG = L_G + 32;
constexpr int L_END = L_BIG + NW * 2 * DM;

__device__ __forceinline__ float bf16r(float x) { return (float)(__bf16)x; }
__device__ __forceinline__ float lo16(u32 w) { return __uint_as_float(w << 16); }
__device__ __forceinline__ float hi16(u32 w) { return __uint_as_float(w & 0xffff0000u); }
__device__ __forceinline__ float dot2(u32 a, u32 b, float c) {
  return __builtin_amdgcn_fdot2_f32_bf16(__builtin_bit_cast(bf16x2, a), __builtin_bit_cast(bf16x2, b), c, false);
}
template <int CTRL> __device__ __forceinline__ float dpp(float x) {
  return __builtin_bit_cast(float, __builtin_amdgcn_mov_dpp(__builtin_bit_cast(int, x), CTRL, 0xf, 0xf, true));
}
constexpr int XOR1 = 0xB1, XOR2 = 0x4E, XOR7 = 0x141, XOR8 = 0x128, ROR4 = 0x124;
__device__ __forceinline__ float row_sum16(float x) {
  x += dpp<XOR1>(x); x += dpp<XOR2>(x); x += dpp<XOR7>(x); x += dpp<XOR8>(x); return x;
}
__device__ __forceinline__ float row_sum_q4(float x) {
  x += dpp<ROR4>(x); x += dpp<XOR8>(x); return x;
}
__device__ __forceinline__ float row_max_q4(float x) {
  x = fmaxf(x, dpp<ROR4>(x)); x = fmaxf(x, dpp<XOR8>(x)); return x;
}
__device__ __forceinline__ float xrow_max(float x) {
  auto s = __builtin_amdgcn_permlane16_swap(__float_as_uint(x), __float_as_uint(x), false, false);
  x = fmaxf(__uint_as_float(s[0]), __uint_as_float(s[1]));
  auto t = __builtin_amdgcn_permlane32_swap(__float_as_uint(x), __float_as_uint(x), false, false);
  return fmaxf(__uint_as_float(t[0]), __uint_as_float(t[1]));
}
__device__ __forceinline__ float xrow_sum(float x) {
  auto s = __builtin_amdgcn_permlane16_swap(__float_as_uint(x), __float_as_uint(x), false, false);
  x = __uint_as_float(s[0]) + __uint_as_float(s[1]);
  auto t = __builtin_amdgcn_permlane32_swap(__float_as_uint(x), __float_as_uint(x), false, false);
  return __uint_as_float(t[0]) + __uint_as_float(t[1]);
}
__device__ __forceinline__ float wave_sum(float x) { return xrow_sum(row_sum16(x)); }
__device__ __forceinline__ u32x4 ldnt(const void* p) { return __builtin_nontemporal_load(reinterpret_cast<const u32x4*>(p)); }
__device__ __forceinline__ u32x4 ld16(const void* p) { return *reinterpret_cast<const u32x4*>(p); }
__device__ __forceinline__ void put_granule(u64* g, u32 tag, float v) {
  __hip_atomic_store((gu64*)g, ((u64)tag << 32) | (u64)__float_as_uint(v), RLX_AGENT);
}
__device__ __forceinline__ u64 get_granule(const u64* g) { return __hip_atomic_load((gu64*)g, RLX_AGENT); }

struct Stage { u32x4 k[T]; u32x4 v[T]; };

__device__ __forceinline__ void issue(Stage& s, const char* kp, const char* vp) {
#pragma unroll
  for (int t = 0; t < T; ++t) s.k[t] = ldnt(kp + t * 1024);
#pragma unroll
  for (int t = 0; t < T; ++t) s.v[t] = ldnt(vp + t * 1024);
  __builtin_amdgcn_sched_barrier(0);
}

__device__ __forceinline__ void consume(const Stage& s, const u32x4 q, float& m, float& l, float (&acc)[8]) {
  constexpr float SCALE = 0.08838834764831845f, LOG2E = 1.4426950408889634f;
  float y[T];
#pragma unroll
  for (int t = 0; t < T; ++t) {
    float d = dot2(s.k[t][0], q[0], 0.f);
    d = dot2(s.k[t][1], q[1], d); d = dot2(s.k[t][2], q[2], d); d = dot2(s.k[t][3], q[3], d);
    d = row_sum16(d);
    y[t] = bf16r(bf16r(d) * SCALE) * LOG2E;
  }
  float mn = m;
#pragma unroll
  for (int t = 0; t < T; ++t) mn = fmaxf(mn, y[t]);
  const float alpha = __builtin_amdgcn_exp2f(m - mn);
  float ps = 0.f;
#pragma unroll
  for (int e = 0; e < 8; ++e) acc[e] *= alpha;
#pragma unroll
  for (int t = 0; t < T; ++t) {
    const float p = __builtin_amdgcn_exp2f(y[t] - mn);
    ps += p;
#pragma unroll
    for (int e = 0; e < 4; ++e) {
      acc[2 * e] = fmaf(p, lo16(s.v[t][e]), acc[2 * e]);
      acc[2 * e + 1] = fmaf(p, hi16(s.v[t][e]), acc[2 * e + 1]);
    }
  }
  l = fmaf(l, alpha, ps);
  m = mn;
  asm volatile("" : "+v"(acc[0]), "+v"(acc[1]), "+v"(acc[2]), "+v"(acc[3]), "+v"(acc[4]), "+v"(acc[5]), "+v"(acc[6]), "+v"(acc[7]), "+v"(l), "+v"(m));
  __builtin_amdgcn_sched_barrier(0);
}

__global__ void __launch_bounds__(NTHR)
fused_decode_kernel(const unsigned short* __restrict__ x, const char* __restrict__ kc, const char* __restrict__ vc,
                    const char* __restrict__ wq, const char* __restrict__ wo, const char* __restrict__ wg,
                    const char* __restrict__ wu, const char* __restrict__ wd, unsigned short* __restrict__ out,
                    char* __restrict__ ws) {
  __shared__ __attribute__((aligned(16))) float lds[L_END];
  const int tid = threadIdx.x, lane = tid & 63, wave = __builtin_amdgcn_readfirstlane(tid >> 6);
  const int r = lane >> 4, c = lane & 15;
  const int id = blockIdx.x, xcd = id & 7, rest = id >> 3;
  const int h = xcd + 8 * (rest >> 3), b = (rest >> 2) & 1, j = rest & 3;
  const int grp = (rest >> 2) * 8 + xcd;
  const int prod = (b * NH + h) * 4 + j;
  const bool grp_ffn = grp < NSLICE;
  const bool grp_red = grp >= NSLICE && grp < NSLICE + NRED / 2;
  const bool is_ffn = grp_ffn && j == 0;
  const bool is_red = grp_red && j < 2;
  const int red = (grp - NSLICE) * 2 + j;

  constexpr int BASE = UNITS / 4;
  int u0, un;
  if (grp_ffn) { constexpr int n = BASE + FFN_OFF / 4; un = (j == 0) ? n - FFN_OFF : n; u0 = (j == 0) ? 0 : j * n - FFN_OFF; }
  else if (grp_red) { constexpr int n = BASE + RED_OFF / 2; un = (j < 2) ? n - RED_OFF : n; u0 = (j < 2) ? j * (n - RED_OFF) : j * n - 2 * RED_OFF; }
  else { un = BASE; u0 = j * BASE; }
  const int cnt = (un - wave + NW - 1) / NW;

  const u32x4 xr = ld16((const char*)x + (wave & 1) * 1024 + lane * 16);
  const u32 tag = *(const u32*)(ws + WS_EPOCH) + 1u;
  u32x4 wqr[16];
  {
    const char* p = wq + (size_t)(64 * wave + r) * (NH * DH * 2) + h * (DH * 2) + c * 16;
#pragma unroll
    for (int i = 0; i < 16; ++i) wqr[i] = ld16(p + (size_t)i * 4 * (NH * DH * 2));
  }
  __builtin_amdgcn_sched_barrier(0);
  const size_t kvoff = ((size_t)(b * NH + h) * SK + (size_t)(u0 + wave) * UKEYS) * (DH * 2) + lane * 16;
  const char* kp = kc + kvoff;
  const char* vp = vc + kvoff;
  constexpr size_t STEP = (size_t)NW * UKEYS * DH * 2;
  Stage A, B;
  issue(A, kp, vp);
  issue(B, kp + STEP, vp + STEP);

  {
    float xv[8];
#pragma unroll
    for (int e = 0; e < 4; ++e) { xv[2 * e] = lo16(xr[e]); xv[2 * e + 1] = hi16(xr[e]); }
    float s = 0.f;
#pragma unroll
    for (int e = 0; e < 8; ++e) s += xv[e];
    const float mean = wave_sum(s) * (1.f / DM);
    float s2 = 0.f;
#pragma unroll
    for (int e = 0; e < 8; ++e) { const float d = xv[e] - mean; s2 = fmaf(d, d, s2); }
    const float var = bf16r(wave_sum(s2) * (1.f / DM));
    const float mb = bf16r(mean);
    const float den = bf16r(sqrtf(bf16r(var + 1e-5f)));
    f32x4 o0, o1;
#pragma unroll
    for (int e = 0; e < 4; ++e) { o0[e] = bf16r(bf16r(xv[e] - mb) / den); o1[e] = bf16r(bf16r(xv[4 + e] - mb) / den); }
    if (wave < 2) {
      *reinterpret_cast<f32x4*>(&lds[L_XN + wave * DM + lane * 8]) = o0;
      *reinterpret_cast<f32x4*>(&lds[L_XN + wave * DM + lane * 8 + 4]) = o1;
    }
  }
  __syncthreads();
  {
    float qa[8];
#pragma unroll
    for (int e = 0; e < 8; ++e) qa[e] = 0.f;
#pragma unroll
    for (int i = 0; i < 16; ++i) {
      const float xv = lds[L_XN + b * DM + 64 * wave + r + 4 * i];
#pragma unroll
      for (int e = 0; e < 4; ++e) { qa[2 * e] = fmaf(xv, lo16(wqr[i][e]), qa[2 * e]); qa[2 * e + 1] = fmaf(xv, hi16(wqr[i][e]), qa[2 * e + 1]); }
    }
#pragma unroll
    for (int e = 0; e < 8; ++e) qa[e] = xrow_sum(qa[e]);
    if (r == 0) {
      *reinterpret_cast<f32x4*>(&lds[L_QP + wave * DH + 8 * c]) = f32x4{qa[0], qa[1], qa[2], qa[3]};
      *reinterpret_cast<f32x4*>(&lds[L_QP + wave * DH + 8 * c + 4]) = f32x4{qa[4], qa[5], qa[6], qa[7]};
    }
  }
  __syncthreads();
  if (tid < 64) {
    float s0 = 0.f, s1 = 0.f;
#pragma unroll
    for (int w = 0; w < NW; ++w) { const f32x2 t2 = *reinterpret_cast<const f32x2*>(&lds[L_QP + w * DH + 2 * tid]); s0 += t2[0]; s1 += t2[1]; }
    const bf16x2 pk = {(__bf16)s0, (__bf16)s1};
    reinterpret_cast<u32*>(lds)[L_QB + tid] = __builtin_bit_cast(u32, pk);
  }
  __syncthreads();
  const u32x4 q = *reinterpret_cast<const u32x4*>(&lds[L_QB + 4 * c]);

  u32x4 fg[4], fu[4], fd[4];
  if (is_ffn) {
    const size_t o = (size_t)(tid >> 2) * (FF * 2) + (size_t)grp * (FS * 2) + (tid & 3) * 16;
#pragma unroll
    for (int i = 0; i < 4; ++i) { fg[i] = ld16(wg + o + (size_t)i * 128 * FF * 2); fu[i] = ld16(wu + o + (size_t)i * 128 * FF * 2); }
    const size_t od = (size_t)(grp * FS + 4 * wave) * (DM * 2) + lane * 16;
#pragma unroll
    for (int i = 0; i < 4; ++i) fd[i] = ld16(wd + od + i * (DM * 2));
  }
  __builtin_amdgcn_sched_barrier(0);

  float m = -INFINITY, l = 0.f, acc[8];
#pragma unroll
  for (int e = 0; e < 8; ++e) acc[e] = 0.f;

  consume(A, q, m, l, acc); issue(A, kp + 2 * STEP, vp + 2 * STEP);
  consume(B, q, m, l, acc); issue(B, kp + 3 * STEP, vp + 3 * STEP);

  if (is_ffn) {
    const int row = tid >> 2;
#pragma unroll
    for (int bb = 0; bb < 2; ++bb) {
      float ga[8], ua[8];
#pragma unroll
      for (int e = 0; e < 8; ++e) { ga[e] = 0.f; ua[e] = 0.f; }
#pragma unroll
      for (int i = 0; i < 4; ++i) {
        const float xv = lds[L_XN + bb * DM + row + 128 * i];
#pragma unroll
        for (int e = 0; e < 4; ++e) {
          ga[2 * e] = fmaf(xv, lo16(fg[i][e]), ga[2 * e]); ga[2 * e + 1] = fmaf(xv, hi16(fg[i][e]), ga[2 * e + 1]);
          ua[2 * e] = fmaf(xv, lo16(fu[i][e]), ua[2 * e]); ua[2 * e + 1] = fmaf(xv, hi16(fu[i][e]), ua[2 * e + 1]);
        }
      }
#pragma unroll
      for (int e = 0; e < 8; ++e) { ga[e] = xrow_sum(row_sum_q4(ga[e])); ua[e] = xrow_sum(row_sum_q4(ua[e])); }
      if (lane < 4) {
        float* rec = &lds[L_BIG + wave * 128 + bb * 64 + lane * 8];
        *reinterpret_cast<f32x4*>(rec) = f32x4{ga[0], ga[1], ga[2], ga[3]};
        *reinterpret_cast<f32x4*>(rec + 4) = f32x4{ga[4], ga[5], ga[6], ga[7]};
        *reinterpret_cast<f32x4*>(rec + 32) = f32x4{ua[0], ua[1], ua[2], ua[3]};
        *reinterpret_cast<f32x4*>(rec + 36) = f32x4{ua[4], ua[5], ua[6], ua[7]};
      }
    }
    __syncthreads();
    if (tid < 64) {
      const int bb = tid >> 5, col = tid & 31;
      float g = 0.f, u = 0.f;
#pragma unroll
      for (int w = 0; w < NW; ++w) { g += lds[L_BIG + w * 128 + bb * 64 + col]; u += lds[L_BIG + w * 128 + bb * 64 + 32 + col]; }
      g = bf16r(g); u = bf16r(u);
      const float inner = 0.7978845608028654f * (g + 0.044715f * g * g * g);
      const float ge = bf16r(0.5f * g * (1.f + tanhf(inner)));
      lds[L_HS + tid] = bf16r(ge * u);
    }
    __syncthreads();
#pragma unroll
    for (int bb = 0; bb < 2; ++bb) {
      float da[8];
#pragma unroll
      for (int e = 0; e < 8; ++e) da[e] = 0.f;
#pragma unroll
      for (int i = 0; i < 4; ++i) {
        const float hv = lds[L_HS + bb * 32 + 4 * wave + i];
#pragma unroll
        for (int e = 0; e < 4; ++e) { da[2 * e] = fmaf(hv, lo16(fd[i][e]), da[2 * e]); da[2 * e + 1] = fmaf(hv, hi16(fd[i][e]), da[2 * e + 1]); }
      }
      float* rec = &lds[L_BIG + (wave * 2 + bb) * DM + lane * 8];
      *reinterpret_cast<f32x4*>(rec) = f32x4{da[0], da[1], da[2], da[3]};
      *reinterpret_cast<f32x4*>(rec + 4) = f32x4{da[4], da[5], da[6], da[7]};
    }
    __syncthreads();
#pragma unroll
    for (int bb = 0; bb < 2; ++bb) {
      float s = 0.f;
#pragma unroll
      for (int w = 0; w < NW; ++w) s += lds[L_BIG + (w * 2 + bb) * DM + tid];
      put_granule((u64*)(ws + WS_F) + (size_t)(grp * 2 + bb) * DM + tid, tag, s);
    }
    __builtin_amdgcn_sched_barrier(0);
  }

  int i = 2;
  for (; i + 3 < cnt; i += 2) {
    consume(A, q, m, l, acc); issue(A, kp + (size_t)(i + 2) * STEP, vp + (size_t)(i + 2) * STEP);
    consume(B, q, m, l, acc); issue(B, kp + (size_t)(i + 3) * STEP, vp + (size_t)(i + 3) * STEP);
  }
  u32x4 W[16];
  const char* wop = wo + (size_t)(h * DH + 16 * wave) * (DM * 2) + lane * 16;
  if (cnt - i == 3) { consume(A, q, m, l, acc); issue(A, kp + (size_t)(i + 2) * STEP, vp + (size_t)(i + 2) * STEP); }
#pragma unroll
  for (int d = 0; d < 16; ++d) W[d] = ld16(wop + d * (DM * 2));
  __builtin_amdgcn_sched_barrier(0);
  if (cnt - i == 3) { consume(B, q, m, l, acc); consume(A, q, m, l, acc); }
  else { consume(A, q, m, l, acc); consume(B, q, m, l, acc); }

  {
    const float mw = xrow_max(m);
    const float sc = __builtin_amdgcn_exp2f(m - mw);
    const float lw = xrow_sum(l * sc);
#pragma unroll
    for (int e = 0; e < 8; ++e) acc[e] = xrow_sum(acc[e] * sc);
    if (r == 0) {
      *reinterpret_cast<f32x4*>(&lds[L_QP + wave * DH + 8 * c]) = f32x4{acc[0], acc[1], acc[2], acc[3]};
      *reinterpret_cast<f32x4*>(&lds[L_QP + wave * DH + 8 * c + 4]) = f32x4{acc[4], acc[5], acc[6], acc[7]};
      if (c == 0) { lds[L_MM + wave] = mw; lds[L_LL + wave] = lw; }
    }
  }
  __syncthreads();
  float M, L;
  {
    const f32x4 m0 = *reinterpret_cast<const f32x4*>(&lds[L_MM]), m1 = *reinterpret_cast<const f32x4*>(&lds[L_MM + 4]);
    const f32x4 l0 = *reinterpret_cast<const f32x4*>(&lds[L_LL]), l1 = *reinterpret_cast<const f32x4*>(&lds[L_LL + 4]);
    M = fmaxf(fmaxf(fmaxf(m0[0], m0[1]), fmaxf(m0[2], m0[3])), fmaxf(fmaxf(m1[0], m1[1]), fmaxf(m1[2], m1[3])));
    L = 0.f;
#pragma unroll
    for (int e = 0; e < 4; ++e) L = fmaf(__builtin_amdgcn_exp2f(m0[e] - M), l0[e], L);
#pragma unroll
    for (int e = 0; e < 4; ++e) L = fmaf(__builtin_amdgcn_exp2f(m1[e] - M), l1[e], L);
    const int rec = lane >> 3, pr = lane & 7;
    const float wi = __builtin_amdgcn_exp2f(lds[L_MM + rec] - M);
    const f32x2 a2 = *reinterpret_cast<const f32x2*>(&lds[L_QP + rec * DH + 16 * wave + 2 * pr]);
    float a0 = a2[0] * wi, a1 = a2[1] * wi;
    a0 += dpp<XOR8>(a0); a1 += dpp<XOR8>(a1);
    a0 = xrow_sum(a0); a1 = xrow_sum(a1);
    float tt[8];
#pragma unroll
    for (int e = 0; e < 8; ++e) tt[e] = 0.f;
#pragma unroll
    for (int d = 0; d < 16; ++d) {
      const float ad = __builtin_bit_cast(float, __builtin_amdgcn_readlane(__builtin_bit_cast(int, (d & 1) ? a1 : a0), d >> 1));
#pragma unroll
      for (int e = 0; e < 4; ++e) { tt[2 * e] = fmaf(ad, lo16(W[d][e]), tt[2 * e]); tt[2 * e + 1] = fmaf(ad, hi16(W[d][e]), tt[2 * e + 1]); }
    }
    float* rp = &lds[L_BIG + wave * DM + lane * 8];
    *reinterpret_cast<f32x4*>(rp) = f32x4{tt[0], tt[1], tt[2], tt[3]};
    *reinterpret_cast<f32x4*>(rp + 4) = f32x4{tt[4], tt[5], tt[6], tt[7]};
  }
  __syncthreads();
  {
    float s = 0.f;
#pragma unroll
    for (int w = 0; w < NW; ++w) s += lds[L_BIG + w * DM + tid];
    put_granule((u64*)(ws + WS_T) + (size_t)prod * DM + tid, tag, s);
    if (tid < 2) put_granule((u64*)(ws + WS_ML) + prod * 2 + tid, tag, tid ? L : M);
  }
  if (!is_red) return;

  __syncthreads();
  const int col0 = red * RC;
#pragma unroll
  for (int it = 0; it < 2; ++it) {
    const int e = tid + it * NTHR;
    if (e < NSLICE * 16) {
      const int s = e >> 4, bb = (e >> 3) & 1, pr = e & 7;
      const u64* gp = (const u64*)(ws + WS_F) + (size_t)(s * 2 + bb) * DM + col0 + 2 * pr;
      u64 g0 = 0, g1 = 0;
      bool ok = false;
      for (u32 spins = 0;; ++spins) {
        if (!ok) { g0 = get_granule(gp); g1 = get_granule(gp + 1); ok = (u32)(g0 >> 32) == tag && (u32)(g1 >> 32) == tag; }
        if (__all(ok) || spins > SPIN_CAP) break;
        __builtin_amdgcn_s_sleep(4);
      }
      *reinterpret_cast<f32x2*>(&lds[L_BIG + s * 32 + bb * 16 + 2 * pr]) = f32x2{__uint_as_float((u32)g0), __uint_as_float((u32)g1)};
    }
  }
  __syncthreads();
  if (tid < 32) {
    float d = 0.f;
#pragma unroll
    for (int s = 0; s < NSLICE; ++s) d += lds[L_BIG + s * 32 + tid];
    d = bf16r(d);
    lds[L_G + tid] = bf16r(d / (1.f + __expf(-d)));
  }
  {
    const int p = tid & 255, half = tid >> 8;
    const u64* gp = (const u64*)(ws + WS_T) + (size_t)p * DM + col0 + 8 * half;
    const u64* mp = (const u64*)(ws + WS_ML) + p * 2;
    u64 g[8], gm = 0, gl = 0;
#pragma unroll
    for (int e = 0; e < 8; ++e) g[e] = 0;
    bool ok = false;
    for (u32 spins = 0;; ++spins) {
      if (!ok) {
#pragma unroll
        for (int e = 0; e < 8; ++e) g[e] = get_granule(gp + e);
        gm = get_granule(mp); gl = get_granule(mp + 1);
        ok = (u32)(gm >> 32) == tag && (u32)(gl >> 32) == tag;
#pragma unroll
        for (int e = 0; e < 8; ++e) ok = ok && (u32)(g[e] >> 32) == tag;
      }
      if (__all(ok) || spins > SPIN_CAP) break;
      __builtin_amdgcn_s_sleep(4);
    }
    const float mj = __uint_as_float((u32)gm), lj = __uint_as_float((u32)gl);
    float Mh = fmaxf(mj, dpp<XOR1>(mj)); Mh = fmaxf(Mh, dpp<XOR2>(Mh));
    const float wj = __builtin_amdgcn_exp2f(mj - Mh);
    float Lh = wj * lj; Lh += dpp<XOR1>(Lh); Lh += dpp<XOR2>(Lh);
    const float sc = wj / Lh;
    float v[8];
#pragma unroll
    for (int e = 0; e < 8; ++e) v[e] = wave_sum(sc * __uint_as_float((u32)g[e]));
    if (lane == 0) {
      *reinterpret_cast<f32x4*>(&lds[L_QP + wave * 8]) = f32x4{v[0], v[1], v[2], v[3]};
      *reinterpret_cast<f32x4*>(&lds[L_QP + wave * 8 + 4]) = f32x4{v[4], v[5], v[6], v[7]};
    }
  }
  __syncthreads();
  if (tid < 32) {
    const int bb = tid >> 4, cc = tid & 15;
    const int w0 = (cc >> 3) * 4 + bb * 2;
    const float s = bf16r(lds[L_QP + w0 * 8 + (cc & 7)] + lds[L_QP + (w0 + 1) * 8 + (cc & 7)]);
    const float xv = __uint_as_float((u32)x[bb * DM + col0 + cc] << 16);
    const float o = xv + bf16r(s * lds[L_G + tid]);
    out[bb * DM + col0 + cc] = __builtin_bit_cast(unsigned short, (__bf16)o);
    if (red == 0 && tid == 0) *(u32*)(ws + WS_EPOCH) = tag;
  }
}

extern "C" void kernel_launch(void* const* d_in, const int* in_sizes, int n_in, void* d_out, int out_size, void* d_ws,
                              size_t ws_size, hipStream_t stream) {
  (void)in_sizes; (void)n_in; (void)out_size;
  if (ws_size < WS_END) return;
  fused_decode_kernel<<<NWG, NTHR, 0, stream>>>((const unsigned short*)d_in[0], (const char*)d_in[1], (const char*)d_in[2],
                                                (const char*)d_in[3], (const char*)d_in[4], (const char*)d_in[6],
                                                (const char*)d_in[7], (const char*)d_in[8], (unsigned short*)d_out, (char*)d_ws);
}
```

```cpp
#include <hip/hip_runtime.h>
#include <stdint.h>

typedef unsigned u32;
typedef unsigned long long u64;
typedef u32 u32x4 __attribute__((ext_vector_type(4)));
typedef float f32x4 __attribute__((ext_vector_type(4)));
typedef float f32x2 __attribute__((ext_vector_type(2)));
typedef __bf16 bf16x2 __attribute__((ext_vector_type(2)));
#define GAS __attribute__((address_space(1)))
typedef GAS u64 gu64;
typedef GAS u32 gu32;
#define RLX_AGENT __ATOMIC_RELAXED, __HIP_MEMORY_SCOPE_AGENT

constexpr int DM = 512, NH = 32, SK = 16384, DH = 128, FF = 1376;
constexpr int NW = 8, NTHR = NW * 64, NWG = 256;
constexpr int T = 4;
constexpr int UKEYS = 4 * T;
constexpr int UNITS = SK / UKEYS;
constexpr int NSLICE = 43, FS = 32;
constexpr int NRED = 32, RC = 16;
constexpr int FFN_OFF = 12;
constexpr int RED_OFF = 6;
constexpr u32 SPIN_CAP = 1u << 20;

constexpr size_t WS_EPOCH = 0;
constexpr size_t WS_ML = 4096;
constexpr size_t WS_T = 8192;
constexpr size_t WS_F = WS_T + (size_t)NWG * DM * 8;
constexpr size_t WS_END = WS_F + (size_t)NSLICE * 2 * DM * 8;

constexpr int L_XN = 0;
constexpr int L_QP = L_XN + 2 * DM;
constexpr int L_QB = L_QP + NW * DH;
constexpr int L_MM = L_QB + 64;
constexpr int L_LL = L_MM + 16;
constexpr int L_HS = L_LL + 16;
constexpr int L_G = L_HS + 128;
constexpr int L_CNT = L_G + 32;
constexpr int L_BIG = L_CNT + 4;
constexpr int L_END = L_BIG + NW * 2 * DM;

__device__ __forceinline__ float bf16r(float x) { return (float)(__bf16)x; }
__device__ __forceinline__ float lo16(u32 w) { return __uint_as_float(w << 16); }
__device__ __forceinline__ float hi16(u32 w) { return __uint_as_float(w & 0xffff0000u); }
__device__ __forceinline__ float dot2(u32 a, u32 b, float c) {
  return __builtin_amdgcn_fdot2_f32_bf16(__builtin_bit_cast(bf16x2, a), __builtin_bit_cast(bf16x2, b), c, false);
}
template <int CTRL> __device__ __forceinline__ float dpp(float x) {
  return __builtin_bit_cast(float, __builtin_amdgcn_mov_dpp(__builtin_bit_cast(int, x), CTRL, 0xf, 0xf, true));
}
constexpr int XOR1 = 0xB1, XOR2 = 0x4E, XOR7 = 0x141, XOR8 = 0x128, ROR4 = 0x124;
__device__ __forceinline__ float row_sum16(float x) {
  x += dpp<XOR1>(x); x += dpp<XOR2>(x); x += dpp<XOR7>(x); x += dpp<XOR8>(x); return x;
}
__device__ __forceinline__ float row_sum_q4(float x) {
  x += dpp<ROR4>(x); x += dpp<XOR8>(x); return x;
}
__device__ __forceinline__ float row_max_q4(float x) {
  x = fmaxf(x, dpp<ROR4>(x)); x = fmaxf(x, dpp<XOR8>(x)); return x;
}
__device__ __forceinline__ float xrow_max(float x) {
  auto s = __builtin_amdgcn_permlane16_swap(__float_as_uint(x), __float_as_uint(x), false, false);
  x = fmaxf(__uint_as_float(s[0]), __uint_as_float(s[1]));
  auto t = __builtin_amdgcn_permlane32_swap(__float_as_uint(x), __float_as_uint(x), false, false);
  return fmaxf(__uint_as_float(t[0]), __uint_as_float(t[1]));
}
__device__ __forceinline__ float xrow_sum(float x) {
  auto s = __builtin_amdgcn_permlane16_swap(__float_as_uint(x), __float_as_uint(x), false, false);
  x = __uint_as_float(s[0]) + __uint_as_float(s[1]);
  auto t = __builtin_amdgcn_permlane32_swap(__float_as_uint(x), __float_as_uint(x), false, false);
  return __uint_as_float(t[0]) + __uint_as_float(t[1]);
}
__device__ __forceinline__ float wave_sum(float x) { return xrow_sum(row_sum16(x)); }
__device__ __forceinline__ u32x4 ldnt(const void* p) { return __builtin_nontemporal_load(reinterpret_cast<const u32x4*>(p)); }
__device__ __forceinline__ u32x4 ld16(const void* p) { return *reinterpret_cast<const u32x4*>(p); }
__device__ __forceinline__ void put_granule(u64* g, u32 tag, float v) {
  __hip_atomic_store((gu64*)g, ((u64)tag << 32) | (u64)__float_as_uint(v), RLX_AGENT);
}
__device__ __forceinline__ u64 get_granule(const u64* g) { return __hip_atomic_load((gu64*)g, RLX_AGENT); }

#define STAMP(i) do {} while (0)

struct Stage { u32x4 k[T]; u32x4 v[T]; };

#define STAGE_REGS(s) "+v"(s.k[0]), "+v"(s.k[1]), "+v"(s.k[2]), "+v"(s.k[3]), "+v"(s.v[0]), "+v"(s.v[1]), "+v"(s.v[2]), "+v"(s.v[3])
__device__ __forceinline__ void ld_plain(u32x4& d, u32 voff, const char* base) {
  asm volatile("s_nop 4\n\tglobal_load_dwordx4 %0, %1, %2" : "=&v"(d) : "v"(voff), "s"(base) : "memory");
}
__device__ __forceinline__ void ld_nt(u32x4& d, u32 voff, const char* base) {
  asm volatile("s_nop 4\n\tglobal_load_dwordx4 %0, %1, %2 nt" : "=&v"(d) : "v"(voff), "s"(base) : "memory");
}
__device__ __forceinline__ void issue(Stage& s, u32 voff, const char* kb, const char* vb) {
  asm volatile("s_nop 4\n\t"
               "global_load_dwordx4 %0, %8, %9 nt\n\t"
               "global_load_dwordx4 %1, %8, %9 offset:1024 nt\n\t"
               "global_load_dwordx4 %2, %8, %9 offset:2048 nt\n\t"
               "global_load_dwordx4 %3, %8, %9 offset:3072 nt\n\t"
               "global_load_dwordx4 %4, %8, %10 nt\n\t"
               "global_load_dwordx4 %5, %8, %10 offset:1024 nt\n\t"
               "global_load_dwordx4 %6, %8, %10 offset:2048 nt\n\t"
               "global_load_dwordx4 %7, %8, %10 offset:3072 nt"
               : "=&v"(s.k[0]), "=&v"(s.k[1]), "=&v"(s.k[2]), "=&v"(s.k[3]), "=&v"(s.v[0]), "=&v"(s.v[1]), "=&v"(s.v[2]), "=&v"(s.v[3])
               : "v"(voff), "s"(kb), "s"(vb) : "memory");
}
__device__ __forceinline__ void refill(Stage& s, u32 voff, const char* kb, const char* vb, int t, int un) {
  asm volatile("s_nop 4\n\t"
               "s_cmp_lt_i32 %11, %12\n\t"
               "s_cbranch_scc0 1f\n\t"
               "global_load_dwordx4 %0, %8, %9 nt\n\t"
               "global_load_dwordx4 %1, %8, %9 offset:1024 nt\n\t"
               "global_load_dwordx4 %2, %8, %9 offset:2048 nt\n\t"
               "global_load_dwordx4 %3, %8, %9 offset:3072 nt\n\t"
               "global_load_dwordx4 %4, %8, %10 nt\n\t"
               "global_load_dwordx4 %5, %8, %10 offset:1024 nt\n\t"
               "global_load_dwordx4 %6, %8, %10 offset:2048 nt\n\t"
               "global_load_dwordx4 %7, %8, %10 offset:3072 nt\n\t"
               "s_branch 2f\n"
               "1:\n\t"
               "global_load_dwordx4 %0, %8, %9\n\t"
               "global_load_dwordx4 %1, %8, %9 offset:1024\n\t"
               "global_load_dwordx4 %2, %8, %9 offset:2048\n\t"
               "global_load_dwordx4 %3, %8, %9 offset:3072\n\t"
               "global_load_dwordx4 %4, %8, %10\n\t"
               "global_load_dwordx4 %5, %8, %10 offset:1024\n\t"
               "global_load_dwordx4 %6, %8, %10 offset:2048\n\t"
               "global_load_dwordx4 %7, %8, %10 offset:3072\n"
               "2:"
               : STAGE_REGS(s) : "v"(voff), "s"(kb), "s"(vb), "s"(t), "s"(un) : "memory", "scc");
}
template <int N> __device__ __forceinline__ void wait_k(Stage& s) {
  asm volatile("s_waitcnt vmcnt(%4)" : "+v"(s.k[0]), "+v"(s.k[1]), "+v"(s.k[2]), "+v"(s.k[3]) : "i"(N) : "memory");
}
template <int N> __device__ __forceinline__ void wait_v(Stage& s) {
  asm volatile("s_waitcnt vmcnt(%4)" : "+v"(s.v[0]), "+v"(s.v[1]), "+v"(s.v[2]), "+v"(s.v[3]) : "i"(N) : "memory");
}
template <int N> __device__ __forceinline__ void wait_1(u32x4& d) { asm volatile("s_waitcnt vmcnt(%1)" : "+v"(d) : "i"(N) : "memory"); }

template <int LATER>
__device__ __forceinline__ void consume(Stage& s, const u32x4 q, float& m, float& l, float (&acc)[8]) {
  constexpr float SCALE = 0.08838834764831845f, LOG2E = 1.4426950408889634f;
  float y[T];
  wait_k<LATER + 4>(s);
#pragma unroll
  for (int t = 0; t < T; ++t) {
    float d = dot2(s.k[t][0], q[0], 0.f);
    d = dot2(s.k[t][1], q[1], d); d = dot2(s.k[t][2], q[2], d); d = dot2(s.k[t][3], q[3], d);
    d = row_sum16(d);
    y[t] = bf16r(bf16r(d) * SCALE) * LOG2E;
  }
  float mn = m;
#pragma unroll
  for (int t = 0; t < T; ++t) mn = fmaxf(mn, y[t]);
  const float alpha = __builtin_amdgcn_exp2f(m - mn);
  float ps = 0.f;
#pragma unroll
  for (int e = 0; e < 8; ++e) acc[e] *= alpha;
  wait_v<LATER>(s);
#pragma unroll
  for (int t = 0; t < T; ++t) {
    const float p = __builtin_amdgcn_exp2f(y[t] - mn);
    ps += p;
#pragma unroll
    for (int e = 0; e < 4; ++e) {
      acc[2 * e] = fmaf(p, lo16(s.v[t][e]), acc[2 * e]);
      acc[2 * e + 1] = fmaf(p, hi16(s.v[t][e]), acc[2 * e + 1]);
    }
  }
  l = fmaf(l, alpha, ps);
  m = mn;
  asm volatile("" : "+v"(acc[0]), "+v"(acc[1]), "+v"(acc[2]), "+v"(acc[3]), "+v"(acc[4]), "+v"(acc[5]), "+v"(acc[6]), "+v"(acc[7]), "+v"(l), "+v"(m));
  __builtin_amdgcn_sched_barrier(0);
}

__global__ void __launch_bounds__(NTHR)
fused_decode_kernel(const unsigned short* __restrict__ x, const char* __restrict__ kc, const char* __restrict__ vc,
                    const char* __restrict__ wq, const char* __restrict__ wo, const char* __restrict__ wg,
                    const char* __restrict__ wu, const char* __restrict__ wd, unsigned short* __restrict__ out,
                    char* __restrict__ ws) {
  __shared__ __attribute__((aligned(16))) float lds[L_END];
  const int tid = threadIdx.x, lane = tid & 63, wave = __builtin_amdgcn_readfirstlane(tid >> 6);
  const int r = lane >> 4, c = lane & 15;
  const int id = blockIdx.x, xcd = id & 7, rest = id >> 3;
  const int h = xcd + 8 * (rest >> 3), b = (rest >> 2) & 1, j = rest & 3;
  const int grp = (rest >> 2) * 8 + xcd;
  const int prod = (b * NH + h) * 4 + j;
  const bool grp_ffn = grp < NSLICE;
  const bool grp_red = grp >= NSLICE && grp < NSLICE + NRED / 2;
  const bool is_ffn = grp_ffn && j == 0;
  const bool is_red = grp_red && j < 2;
  const int red = (grp - NSLICE) * 2 + j;

  constexpr int BASE = UNITS / 4;
  int u0, un;
  if (grp_ffn) { constexpr int n = BASE + FFN_OFF / 4; un = (j == 0) ? n - FFN_OFF : n; u0 = (j == 0) ? 0 : j * n - FFN_OFF; }
  else if (grp_red) { constexpr int n = BASE + RED_OFF / 2; un = (j < 2) ? n - RED_OFF : n; u0 = (j < 2) ? j * (n - RED_OFF) : j * n - 2 * RED_OFF; }
  else { un = BASE; u0 = j * BASE; }

  STAMP(0);
  const u32 voff = lane * 16;
  u32x4 xr;
  ld_plain(xr, voff, (const char*)x + (wave & 1) * 1024);
  const u32 tag = *(const u32*)(ws + WS_EPOCH) + 1u;
  u32x4 wqr[16];
  {
    const char* p = wq + (size_t)(64 * wave) * (NH * DH * 2) + h * (DH * 2);
    const u32 vo = r * (NH * DH * 2) + c * 16;
#pragma unroll
    for (int i = 0; i < 16; ++i) ld_plain(wqr[i], vo, p + (size_t)i * 4 * (NH * DH * 2));
  }
  if (tid == 0) reinterpret_cast<u32*>(lds)[L_CNT] = is_ffn ? 2 * NW : 3 * NW;
  __builtin_amdgcn_s_barrier();
  const size_t kvoff = ((size_t)(b * NH + h) * SK + (size_t)u0 * UKEYS) * (DH * 2);
  const char* kp = kc + kvoff;
  const char* vp = vc + kvoff;
  constexpr size_t UB = (size_t)UKEYS * DH * 2;
  Stage A, B, C;
  issue(A, voff, kp + wave * UB, vp + wave * UB);
  issue(B, voff, kp + (NW + wave) * UB, vp + (NW + wave) * UB);
  {
    const u32 vo = is_ffn ? (u32)((tid >> 2) * (FF * 2) + (tid & 3) * 16) : voff;
    const char* pk = is_ffn ? wg + grp * (FS * 2) : kp + (2 * NW + wave) * UB;
    const char* pv = is_ffn ? wu + grp * (FS * 2) : vp + (2 * NW + wave) * UB;
    const size_t st = is_ffn ? (size_t)128 * FF * 2 : 1024;
#pragma unroll
    for (int i = 0; i < 4; ++i) ld_nt(C.k[i], vo, pk + i * st);
#pragma unroll
    for (int i = 0; i < 4; ++i) ld_nt(C.v[i], vo, pv + i * st);
  }
  wait_1<40>(xr);

  {
    float xv[8];
#pragma unroll
    for (int e = 0; e < 4; ++e) { xv[2 * e] = lo16(xr[e]); xv[2 * e + 1] = hi16(xr[e]); }
    float s = 0.f;
#pragma unroll
    for (int e = 0; e < 8; ++e) s += xv[e];
    const float mean = wave_sum(s) * (1.f / DM);
    float s2 = 0.f;
#pragma unroll
    for (int e = 0; e < 8; ++e) { const float d = xv[e] - mean; s2 = fmaf(d, d, s2); }
    const float var = bf16r(wave_sum(s2) * (1.f / DM));
    const float mb = bf16r(mean);
    const float den = bf16r(sqrtf(bf16r(var + 1e-5f)));
    f32x4 o0, o1;
#pragma unroll
    for (int e = 0; e < 4; ++e) { o0[e] = bf16r(bf16r(xv[e] - mb) / den); o1[e] = bf16r(bf16r(xv[4 + e] - mb) / den); }
    if (wave < 2) {
      *reinterpret_cast<f32x4*>(&lds[L_XN + wave * DM + lane * 8]) = o0;
      *reinterpret_cast<f32x4*>(&lds[L_XN + wave * DM + lane * 8 + 4]) = o1;
    }
  }
  __syncthreads();
  {
    float qa[8];
#pragma unroll
    for (int e = 0; e < 8; ++e) qa[e] = 0.f;
#pragma unroll
    for (int i = 0; i < 16; ++i) {
      const float xv = lds[L_XN + b * DM + 64 * wave + r + 4 * i];
      if (i == 0) { wait_1<32>(wqr[0]); wait_1<32>(wqr[1]); wait_1<32>(wqr[2]); wait_1<32>(wqr[3]); wait_1<32>(wqr[4]); wait_1<32>(wqr[5]); wait_1<32>(wqr[6]); wait_1<32>(wqr[7]); }
      if (i == 8) { wait_1<24>(wqr[8]); wait_1<24>(wqr[9]); wait_1<24>(wqr[10]); wait_1<24>(wqr[11]); wait_1<24>(wqr[12]); wait_1<24>(wqr[13]); wait_1<24>(wqr[14]); wait_1<24>(wqr[15]); }
#pragma unroll
      for (int e = 0; e < 4; ++e) { qa[2 * e] = fmaf(xv, lo16(wqr[i][e]), qa[2 * e]); qa[2 * e + 1] = fmaf(xv, hi16(wqr[i][e]), qa[2 * e + 1]); }
    }
#pragma unroll
    for (int e = 0; e < 8; ++e) qa[e] = xrow_sum(qa[e]);
    if (r == 0) {
      *reinterpret_cast<f32x4*>(&lds[L_QP + wave * DH + 8 * c]) = f32x4{qa[0], qa[1], qa[2], qa[3]};
      *reinterpret_cast<f32x4*>(&lds[L_QP + wave * DH + 8 * c + 4]) = f32x4{qa[4], qa[5], qa[6], qa[7]};
    }
  }
  __syncthreads();
  if (tid < 64) {
    float s0 = 0.f, s1 = 0.f;
#pragma unroll
    for (int w = 0; w < NW; ++w) { const f32x2 t2 = *reinterpret_cast<const f32x2*>(&lds[L_QP + w * DH + 2 * tid]); s0 += t2[0]; s1 += t2[1]; }
    const bf16x2 pk = {(__bf16)s0, (__bf16)s1};
    reinterpret_cast<u32*>(lds)[L_QB + tid] = __builtin_bit_cast(u32, pk);
  }
  __syncthreads();
  const u32x4 q = *reinterpret_cast<const u32x4*>(&lds[L_QB + 4 * c]);

  STAMP(1);
  float m = -INFINITY, l = 0.f, acc[8];
#pragma unroll
  for (int e = 0; e < 8; ++e) acc[e] = 0.f;
  auto grab = [&]() -> int {
    u32 t = 0;
    if (lane == 0) t = __hip_atomic_fetch_add(&reinterpret_cast<u32*>(lds)[L_CNT], 1u, __ATOMIC_RELAXED, __HIP_MEMORY_SCOPE_WORKGROUP);
    return (int)__builtin_amdgcn_readfirstlane(t);
  };
  u32x4 fd[4];
  if (is_ffn) {
#pragma unroll
    for (int i = 0; i < 4; ++i) ld_nt(fd[i], voff, wd + (size_t)(grp * FS + 4 * wave + i) * (DM * 2));
  }
  {
    const int t0 = grab(); consume<16>(A, q, m, l, acc); refill(A, voff, kp + t0 * UB, vp + t0 * UB, t0, un);
    const int t1 = grab(); consume<16>(B, q, m, l, acc); refill(B, voff, kp + t1 * UB, vp + t1 * UB, t1, un);
  }

  if (is_ffn) {
    wait_k<16>(C); wait_v<16>(C);
    wait_1<16>(fd[0]); wait_1<16>(fd[1]); wait_1<16>(fd[2]); wait_1<16>(fd[3]);
    const int row = tid >> 2;
#pragma unroll
    for (int bb = 0; bb < 2; ++bb) {
      float ga[8], ua[8];
#pragma unroll
      for (int e = 0; e < 8; ++e) { ga[e] = 0.f; ua[e] = 0.f; }
#pragma unroll
      for (int i = 0; i < 4; ++i) {
        const float xv = lds[L_XN + bb * DM + row + 128 * i];
#pragma unroll
        for (int e = 0; e < 4; ++e) {
          ga[2 * e] = fmaf(xv, lo16(C.k[i][e]), ga[2 * e]); ga[2 * e + 1] = fmaf(xv, hi16(C.k[i][e]), ga[2 * e + 1]);
          ua[2 * e] = fmaf(xv, lo16(C.v[i][e]), ua[2 * e]); ua[2 * e + 1] = fmaf(xv, hi16(C.v[i][e]), ua[2 * e + 1]);
        }
      }
#pragma unroll
      for (int e = 0; e < 8; ++e) { ga[e] = xrow_sum(row_sum_q4(ga[e])); ua[e] = xrow_sum(row_sum_q4(ua[e])); }
      if (lane < 4) {
        float* rec = &lds[L_BIG + wave * 128 + bb * 64 + lane * 8];
        *reinterpret_cast<f32x4*>(rec) = f32x4{ga[0], ga[1], ga[2], ga[3]};
        *reinterpret_cast<f32x4*>(rec + 4) = f32x4{ga[4], ga[5], ga[6], ga[7]};
        *reinterpret_cast<f32x4*>(rec + 32) = f32x4{ua[0], ua[1], ua[2], ua[3]};
        *reinterpret_cast<f32x4*>(rec + 36) = f32x4{ua[4], ua[5], ua[6], ua[7]};
      }
    }
    __syncthreads();
    if (tid < 64) {
      const int bb = tid >> 5, col = tid & 31;
      float g = 0.f, u = 0.f;
#pragma unroll
      for (int w = 0; w < NW; ++w) { g += lds[L_BIG + w * 128 + bb * 64 + col]; u += lds[L_BIG + w * 128 + bb * 64 + 32 + col]; }
      g = bf16r(g); u = bf16r(u);
      const float inner = 0.7978845608028654f * (g + 0.044715f * g * g * g);
      const float ge = bf16r(0.5f * g * (1.f + tanhf(inner)));
      lds[L_HS + tid] = bf16r(ge * u);
    }
    __syncthreads();
#pragma unroll
    for (int bb = 0; bb < 2; ++bb) {
      float da[8];
#pragma unroll
      for (int e = 0; e < 8; ++e) da[e] = 0.f;
#pragma unroll
      for (int i = 0; i < 4; ++i) {
        const float hv = lds[L_HS + bb * 32 + 4 * wave + i];
#pragma unroll
        for (int e = 0; e < 4; ++e) { da[2 * e] = fmaf(hv, lo16(fd[i][e]), da[2 * e]); da[2 * e + 1] = fmaf(hv, hi16(fd[i][e]), da[2 * e + 1]); }
      }
      float* rec = &lds[L_BIG + (wave * 2 + bb) * DM + lane * 8];
      *reinterpret_cast<f32x4*>(rec) = f32x4{da[0], da[1], da[2], da[3]};
      *reinterpret_cast<f32x4*>(rec + 4) = f32x4{da[4], da[5], da[6], da[7]};
    }
    __syncthreads();
#pragma unroll
    for (int bb = 0; bb < 2; ++bb) {
      float s = 0.f;
#pragma unroll
      for (int w = 0; w < NW; ++w) s += lds[L_BIG + (w * 2 + bb) * DM + tid];
      put_granule((u64*)(ws + WS_F) + (size_t)(grp * 2 + bb) * DM + tid, tag, s);
    }
    __builtin_amdgcn_sched_barrier(0);
  } else {
    consume<16>(C, q, m, l, acc);
  }

  STAMP(2);
  const char* wop = wo + (size_t)(h * DH + 16 * wave) * (DM * 2);
  int fails = 0, a_first = 1;
  for (;;) {
    const int t0 = grab();
    consume<8>(A, q, m, l, acc);
    { const bool kv = t0 < un; const char* wp = wop + fails * 8192;
      refill(A, voff, kv ? kp + t0 * UB : wp, kv ? vp + t0 * UB : wp + 4096, t0, un); fails += kv ? 0 : 1; }
    if (fails == 2) { a_first = 0; break; }
    const int t1 = grab();
    consume<8>(B, q, m, l, acc);
    { const bool kv = t1 < un; const char* wp = wop + fails * 8192;
      refill(B, voff, kv ? kp + t1 * UB : wp, kv ? vp + t1 * UB : wp + 4096, t1, un); fails += kv ? 0 : 1; }
    if (fails == 2) break;
  }

  STAMP(3);
  {
    const float mw = xrow_max(m);
    const float sc = __builtin_amdgcn_exp2f(m - mw);
    const float lw = xrow_sum(l * sc);
#pragma unroll
    for (int e = 0; e < 8; ++e) acc[e] = xrow_sum(acc[e] * sc);
    if (r == 0) {
      *reinterpret_cast<f32x4*>(&lds[L_QP + wave * DH + 8 * c]) = f32x4{acc[0], acc[1], acc[2], acc[3]};
      *reinterpret_cast<f32x4*>(&lds[L_QP + wave * DH + 8 * c + 4]) = f32x4{acc[4], acc[5], acc[6], acc[7]};
      if (c == 0) { lds[L_MM + wave] = mw; lds[L_LL + wave] = lw; }
    }
  }
  __syncthreads();
  float M, L;
  {
    const f32x4 m0 = *reinterpret_cast<const f32x4*>(&lds[L_MM]), m1 = *reinterpret_cast<const f32x4*>(&lds[L_MM + 4]);
    const f32x4 l0 = *reinterpret_cast<const f32x4*>(&lds[L_LL]), l1 = *reinterpret_cast<const f32x4*>(&lds[L_LL + 4]);
    M = fmaxf(fmaxf(fmaxf(m0[0], m0[1]), fmaxf(m0[2], m0[3])), fmaxf(fmaxf(m1[0], m1[1]), fmaxf(m1[2], m1[3])));
    L = 0.f;
#pragma unroll
    for (int e = 0; e < 4; ++e) L = fmaf(__builtin_amdgcn_exp2f(m0[e] - M), l0[e], L);
#pragma unroll
    for (int e = 0; e < 4; ++e) L = fmaf(__builtin_amdgcn_exp2f(m1[e] - M), l1[e], L);
    const int rec = lane >> 3, pr = lane & 7;
    const float wi = __builtin_amdgcn_exp2f(lds[L_MM + rec] - M);
    const f32x2 a2 = *reinterpret_cast<const f32x2*>(&lds[L_QP + rec * DH + 16 * wave + 2 * pr]);
    float a0 = a2[0] * wi, a1 = a2[1] * wi;
    a0 += dpp<XOR8>(a0); a1 += dpp<XOR8>(a1);
    a0 = xrow_sum(a0); a1 = xrow_sum(a1);
    float tt[8];
#pragma unroll
    for (int e = 0; e < 8; ++e) tt[e] = 0.f;
    wait_k<0>(A); wait_v<0>(A); wait_k<0>(B); wait_v<0>(B);
    const int dA = a_first ? 0 : 4, dB = 4 - dA;
#pragma unroll
    for (int d = 0; d < 8; ++d) {
      const float aA = __builtin_bit_cast(float, __builtin_amdgcn_readlane(__builtin_bit_cast(int, (d & 1) ? a1 : a0), dA + (d >> 1)));
      const float aB = __builtin_bit_cast(float, __builtin_amdgcn_readlane(__builtin_bit_cast(int, (d & 1) ? a1 : a0), dB + (d >> 1)));
      const u32x4 wA = d < 4 ? A.k[d & 3] : A.v[d & 3], wB = d < 4 ? B.k[d & 3] : B.v[d & 3];
#pragma unroll
      for (int e = 0; e < 4; ++e) {
        tt[2 * e] = fmaf(aA, lo16(wA[e]), tt[2 * e]); tt[2 * e + 1] = fmaf(aA, hi16(wA[e]), tt[2 * e + 1]);
        tt[2 * e] = fmaf(aB, lo16(wB[e]), tt[2 * e]); tt[2 * e + 1] = fmaf(aB, hi16(wB[e]), tt[2 * e + 1]);
      }
    }
    float* rp = &lds[L_BIG + wave * DM + lane * 8];
    *reinterpret_cast<f32x4*>(rp) = f32x4{tt[0], tt[1], tt[2], tt[3]};
    *reinterpret_cast<f32x4*>(rp + 4) = f32x4{tt[4], tt[5], tt[6], tt[7]};
  }
  __syncthreads();
  {
    float s = 0.f;
#pragma unroll
    for (int w = 0; w < NW; ++w) s += lds[L_BIG + w * DM + tid];
    put_granule((u64*)(ws + WS_T) + (size_t)prod * DM + tid, tag, s);
    if (tid < 2) put_granule((u64*)(ws + WS_ML) + prod * 2 + tid, tag, tid ? L : M);
  }
  STAMP(4);
  if (!is_red) return;

  __syncthreads();
  const int col0 = red * RC;
#pragma unroll
  for (int it = 0; it < 2; ++it) {
    const int e = tid + it * NTHR;
    if (e < NSLICE * 16) {
      const int s = e >> 4, bb = (e >> 3) & 1, pr = e & 7;
      const u64* gp = (const u64*)(ws + WS_F) + (size_t)(s * 2 + bb) * DM + col0 + 2 * pr;
      u64 g0 = 0, g1 = 0;
      bool ok = false;
      for (u32 spins = 0;; ++spins) {
        if (!ok) { g0 = get_granule(gp); g1 = get_granule(gp + 1); ok = (u32)(g0 >> 32) == tag && (u32)(g1 >> 32) == tag; }
        if (__all(ok) || spins > SPIN_CAP) break;
        __builtin_amdgcn_s_sleep(4);
      }
      *reinterpret_cast<f32x2*>(&lds[L_BIG + s * 32 + bb * 16 + 2 * pr]) = f32x2{__uint_as_float((u32)g0), __uint_as_float((u32)g1)};
    }
  }
  __syncthreads();
  if (tid < 32) {
    float d = 0.f;
#pragma unroll
    for (int s = 0; s < NSLICE; ++s) d += lds[L_BIG + s * 32 + tid];
    d = bf16r(d);
    lds[L_G + tid] = bf16r(d / (1.f + __expf(-d)));
  }
  STAMP(5);
  {
    const int p = tid & 255, half = tid >> 8;
    const u64* gp = (const u64*)(ws + WS_T) + (size_t)p * DM + col0 + 8 * half;
    const u64* mp = (const u64*)(ws + WS_ML) + p * 2;
    u64 g[8], gm = 0, gl = 0;
#pragma unroll
    for (int e = 0; e < 8; ++e) g[e] = 0;
    bool ok = false;
    for (u32 spins = 0;; ++spins) {
      if (!ok) {
#pragma unroll
        for (int e = 0; e < 8; ++e) g[e] = get_granule(gp + e);
        gm = get_granule(mp); gl = get_granule(mp + 1);
        ok = (u32)(gm >> 32) == tag && (u32)(gl >> 32) == tag;
#pragma unroll
        for (int e = 0; e < 8; ++e) ok = ok && (u32)(g[e] >> 32) == tag;
      }
      if (__all(ok) || spins > SPIN_CAP) break;
      __builtin_amdgcn_s_sleep(4);
    }
    const float mj = __uint_as_float((u32)gm), lj = __uint_as_float((u32)gl);
    float Mh = fmaxf(mj, dpp<XOR1>(mj)); Mh = fmaxf(Mh, dpp<XOR2>(Mh));
    const float wj = __builtin_amdgcn_exp2f(mj - Mh);
    float Lh = wj * lj; Lh += dpp<XOR1>(Lh); Lh += dpp<XOR2>(Lh);
    const float sc = wj / Lh;
    float v[8];
#pragma unroll
    for (int e = 0; e < 8; ++e) v[e] = wave_sum(sc * __uint_as_float((u32)g[e]));
    if (lane == 0) {
      *reinterpret_cast<f32x4*>(&lds[L_QP + wave * 8]) = f32x4{v[0], v[1], v[2], v[3]};
      *reinterpret_cast<f32x4*>(&lds[L_QP + wave * 8 + 4]) = f32x4{v[4], v[5], v[6], v[7]};
    }
  }
  __syncthreads();
  if (tid < 32) {
    const int bb = tid >> 4, cc = tid & 15;
    const int w0 = (cc >> 3) * 4 + bb * 2;
    const float s = bf16r(lds[L_QP + w0 * 8 + (cc & 7)] + lds[L_QP + (w0 + 1) * 8 + (cc & 7)]);
    const float xv = __uint_as_float((u32)x[bb * DM + col0 + cc] << 16);
    const float o = xv + bf16r(s * lds[L_G + tid]);
    out[bb * DM + col0 + cc] = __builtin_bit_cast(unsigned short, (__bf16)o);
    if (red == 0 && tid == 0) *(u32*)(ws + WS_EPOCH) = tag;
    STAMP(6);
  }
}

extern "C" void kernel_launch(void* const* d_in, const int* in_sizes, int n_in, void* d_out, int out_size, void* d_ws,
                              size_t ws_size, hipStream_t stream) {
  (void)in_sizes; (void)n_in; (void)out_size;
  if (ws_size < WS_END) return;
  fused_decode_kernel<<<NWG, NTHR, 0, stream>>>((const unsigned short*)d_in[0], (const char*)d_in[1], (const char*)d_in[2],
                                                (const char*)d_in[3], (const char*)d_in[4], (const char*)d_in[6],
                                                (const char*)d_in[7], (const char*)d_in[8], (unsigned short*)d_out, (char*)d_ws);
}
```

```cpp
#include <hip/hip_runtime.h>
#include <stdint.h>

typedef unsigned u32;
typedef unsigned long long u64;
typedef u32 u32x4 __attribute__((ext_vector_type(4)));
typedef u32 u32x2 __attribute__((ext_vector_type(2)));
typedef float f32x4 __attribute__((ext_vector_type(4)));
typedef float f32x2 __attribute__((ext_vector_type(2)));
typedef __bf16 bf16x2 __attribute__((ext_vector_type(2)));

constexpr int DM = 512, NH = 32, SK = 16384, DH = 128, FF = 1376;
constexpr int NW = 8, NTHR = NW * 64, NWG = 256;
constexpr int UKEYS = 16;
constexpr int UPB = SK / UKEYS;
constexpr int UEND = 2 * UPB;
constexpr int NSTAT = 4;
constexpr int NSLICE = 43, FS = 32;
constexpr int NRED = 64;
constexpr u32 SPIN_CAP = 1u << 20;
#define LD_WQ ld_nt
#define HOLD 100
#define POLL_AT 8

constexpr size_t WS_EPOCH = 0;
constexpr size_t WS_CNT = 4096;
constexpr size_t WS_Q = 16384;
constexpr size_t WS_ML = WS_Q + 2 * NH * 64 * 8;
constexpr size_t WS_P = WS_ML + NH * 2 * 8 * 16;
constexpr size_t WS_T = WS_P + (size_t)NH * 8 * 2 * 8 * 128;
constexpr size_t WS_F = WS_T + (size_t)NRED * NWG * 128;
constexpr size_t WS_END = WS_F + (size_t)NRED * NSLICE * 128;

constexpr int L_XN = 0;
constexpr int L_REC = L_XN + 2 * DM;
constexpr int L_MM = L_REC + 2 * NW * DH;
constexpr int L_LL = L_MM + 16;
constexpr int L_O = L_LL + 16;
constexpr int L_HS = L_O + 32;
constexpr int L_CNT = L_HS + 64;
constexpr int L_FR = L_CNT + 4;
constexpr int L_BIG = L_FR + 512;
constexpr int L_END = L_BIG + 4 * 2 * DM;

__device__ __forceinline__ float bf16r(float x) { return (float)(__bf16)x; }
__device__ __forceinline__ float lo16(u32 w) { return __uint_as_float(w << 16); }
__device__ __forceinline__ float hi16(u32 w) { return __uint_as_float(w & 0xffff0000u); }
__device__ __forceinline__ float dot2(u32 a, u32 b, float c) {
  return __builtin_amdgcn_fdot2_f32_bf16(__builtin_bit_cast(bf16x2, a), __builtin_bit_cast(bf16x2, b), c, false);
}
template <int CTRL> __device__ __forceinline__ float dpp(float x) {
  return __builtin_bit_cast(float, __builtin_amdgcn_mov_dpp(__builtin_bit_cast(int, x), CTRL, 0xf, 0xf, true));
}
constexpr int XOR1 = 0xB1, XOR2 = 0x4E, XOR7 = 0x141, XOR8 = 0x128, ROR4 = 0x124;
__device__ __forceinline__ float row_sum16(float x) {
  x += dpp<XOR1>(x); x += dpp<XOR2>(x); x += dpp<XOR7>(x); x += dpp<XOR8>(x); return x;
}
__device__ __forceinline__ float row_sum_q4(float x) {
  x += dpp<ROR4>(x); x += dpp<XOR8>(x); return x;
}
__device__ __forceinline__ float xrow_max(float x) {
  auto s = __builtin_amdgcn_permlane16_swap(__float_as_uint(x), __float_as_uint(x), false, false);
  x = fmaxf(__uint_as_float(s[0]), __uint_as_float(s[1]));
  auto t = __builtin_amdgcn_permlane32_swap(__float_as_uint(x), __float_as_uint(x), false, false);
  return fmaxf(__uint_as_float(t[0]), __uint_as_float(t[1]));
}
__device__ __forceinline__ float xrow_sum(float x) {
  auto s = __builtin_amdgcn_permlane16_swap(__float_as_uint(x), __float_as_uint(x), false, false);
  x = __uint_as_float(s[0]) + __uint_as_float(s[1]);
  auto t = __builtin_amdgcn_permlane32_swap(__float_as_uint(x), __float_as_uint(x), false, false);
  return __uint_as_float(t[0]) + __uint_as_float(t[1]);
}
__device__ __forceinline__ float wave_sum(float x) { return xrow_sum(row_sum16(x)); }

#define STAMPW(i, w) do {} while (0)
#define STAMPU(u) do {} while (0)

struct Stage { u32x4 k[4]; u32x4 v[4]; };

#define STAGE_REGS(s) "+v"(s.k[0]), "+v"(s.k[1]), "+v"(s.k[2]), "+v"(s.k[3]), "+v"(s.v[0]), "+v"(s.v[1]), "+v"(s.v[2]), "+v"(s.v[3])
__device__ __forceinline__ void ld_plain(u32x4& d, u32 voff, const char* base) {
  asm volatile("s_nop 4\n\tglobal_load_dwordx4 %0, %1, %2" : "=&v"(d) : "v"(voff), "s"(base) : "memory");
}
__device__ __forceinline__ void ld_nt2(u32x2& d, u32 voff, const char* base) {
  asm volatile("s_nop 4\n\tglobal_load_dwordx2 %0, %1, %2 nt" : "=&v"(d) : "v"(voff), "s"(base) : "memory");
}
__device__ __forceinline__ void ld_nt(u32x4& d, u32 voff, const char* base) {
  asm volatile("s_nop 4\n\tglobal_load_dwordx4 %0, %1, %2 nt" : "=&v"(d) : "v"(voff), "s"(base) : "memory");
}
__device__ __forceinline__ void ld_dev(u32x4& d, u32 voff, const char* base) {
  asm volatile("s_nop 4\n\tglobal_load_dwordx4 %0, %1, %2 sc1" : "=&v"(d) : "v"(voff), "s"(base) : "memory");
}
__device__ __forceinline__ void st_dev(u32 voff, char* base, float a, float b, u32 tag) {
  const u32x4 d = {__float_as_uint(a), tag, __float_as_uint(b), tag};
  asm volatile("s_nop 4\n\tglobal_store_dwordx4 %0, %1, %2 sc1" :: "v"(voff), "v"(d), "s"(base) : "memory");
}
__device__ __forceinline__ void ticket(u32& t, const char* cnt) {
  asm volatile("s_nop 4\n\ts_mov_b64 exec, 1\n\tglobal_atomic_add %0, %1, %2, %3 sc0\n\ts_mov_b64 exec, -1"
               : "=&v"(t) : "v"(0u), "v"(1u), "s"(cnt) : "memory");
}
template <int N> __device__ __forceinline__ u32 read_ticket(u32 t) {
  u32 v;
  asm volatile("s_waitcnt vmcnt(%2)\n\tv_readfirstlane_b32 %0, %1\n\ts_nop 4" : "=s"(v) : "v"(t), "i"(N) : "memory");
  return v;
}
__device__ __forceinline__ void issue(Stage& s, u32 voff, const char* kb, const char* vb) {
  asm volatile("s_nop 4\n\t"
               "global_load_dwordx4 %0, %8, %9 nt\n\t"
               "global_load_dwordx4 %1, %8, %9 offset:1024 nt\n\t"
               "global_load_dwordx4 %2, %8, %9 offset:2048 nt\n\t"
               "global_load_dwordx4 %3, %8, %9 offset:3072 nt\n\t"
               "global_load_dwordx4 %4, %8, %10 nt\n\t"
               "global_load_dwordx4 %5, %8, %10 offset:1024 nt\n\t"
               "global_load_dwordx4 %6, %8, %10 offset:2048 nt\n\t"
               "global_load_dwordx4 %7, %8, %10 offset:3072 nt"
               : "=&v"(s.k[0]), "=&v"(s.k[1]), "=&v"(s.k[2]), "=&v"(s.k[3]), "=&v"(s.v[0]), "=&v"(s.v[1]), "=&v"(s.v[2]), "=&v"(s.v[3])
               : "v"(voff), "s"(kb), "s"(vb) : "memory");
}
__device__ __forceinline__ void refill(Stage& s, u32& t, u32 voff, const char* kb, const char* vb, const char* cnt, int u) {
  asm volatile("s_nop 4\n\t"
               "s_cmpk_lt_i32 %15, 0x800\n\t"
               "s_cbranch_scc0 1f\n\t"
               "global_load_dwordx4 %0, %9, %10 nt\n\t"
               "global_load_dwordx4 %1, %9, %10 offset:1024 nt\n\t"
               "global_load_dwordx4 %2, %9, %10 offset:2048 nt\n\t"
               "global_load_dwordx4 %3, %9, %10 offset:3072 nt\n\t"
               "global_load_dwordx4 %4, %9, %11 nt\n\t"
               "global_load_dwordx4 %5, %9, %11 offset:1024 nt\n\t"
               "global_load_dwordx4 %6, %9, %11 offset:2048 nt\n\t"
               "global_load_dwordx4 %7, %9, %11 offset:3072 nt\n\t"
               "s_mov_b64 exec, 1\n\t"
               "global_atomic_add %8, %13, %14, %12 sc0\n\t"
               "s_mov_b64 exec, -1\n\t"
               "s_branch 2f\n"
               "1:\n\t"
               "s_waitcnt vmcnt(0)\n"
               "2:"
               : STAGE_REGS(s), "=&v"(t) : "v"(voff), "s"(kb), "s"(vb), "s"(cnt), "v"(0u), "v"(1u), "s"(u) : "memory", "scc");
}
static_assert(UEND == 0x800, "refill compares against 0x800");
template <int N> __device__ __forceinline__ void wait_k(Stage& s) {
  asm volatile("s_waitcnt vmcnt(%4)" : "+v"(s.k[0]), "+v"(s.k[1]), "+v"(s.k[2]), "+v"(s.k[3]) : "i"(N) : "memory");
}
template <int N> __device__ __forceinline__ void wait_v(Stage& s) {
  asm volatile("s_waitcnt vmcnt(%4)" : "+v"(s.v[0]), "+v"(s.v[1]), "+v"(s.v[2]), "+v"(s.v[3]) : "i"(N) : "memory");
}
template <int N, typename V> __device__ __forceinline__ void wait_1(V& d) { asm volatile("s_waitcnt vmcnt(%1)" : "+v"(d) : "i"(N) : "memory"); }
template <int N> __device__ __forceinline__ void wait_n() { asm volatile("s_waitcnt vmcnt(%0)" :: "i"(N) : "memory"); }

template <int LATER>
__device__ __forceinline__ void consume(Stage& s, const u32x4 q, float& m, float& l, float (&acc)[8]) {
  constexpr float SCALE = 0.08838834764831845f, LOG2E = 1.4426950408889634f;
  float y[4];
  wait_k<LATER + 4>(s);
#pragma unroll
  for (int t = 0; t < 4; ++t) {
    float d = dot2(s.k[t][0], q[0], 0.f);
    d = dot2(s.k[t][1], q[1], d); d = dot2(s.k[t][2], q[2], d); d = dot2(s.k[t][3], q[3], d);
    d = row_sum16(d);
    y[t] = bf16r(bf16r(d) * SCALE) * LOG2E;
  }
  float mn = m;
#pragma unroll
  for (int t = 0; t < 4; ++t) mn = fmaxf(mn, y[t]);
  const float alpha = __builtin_amdgcn_exp2f(m - mn);
  float ps = 0.f;
#pragma unroll
  for (int e = 0; e < 8; ++e) acc[e] *= alpha;
  wait_v<LATER>(s);
#pragma unroll
  for (int t = 0; t < 4; ++t) {
    const float p = __builtin_amdgcn_exp2f(y[t] - mn);
    ps += p;
#pragma unroll
    for (int e = 0; e < 4; ++e) {
      acc[2 * e] = fmaf(p, lo16(s.v[t][e]), acc[2 * e]);
      acc[2 * e + 1] = fmaf(p, hi16(s.v[t][e]), acc[2 * e + 1]);
    }
  }
  l = fmaf(l, alpha, ps);
  m = mn;
  asm volatile("" : "+v"(acc[0]), "+v"(acc[1]), "+v"(acc[2]), "+v"(acc[3]), "+v"(acc[4]), "+v"(acc[5]), "+v"(acc[6]), "+v"(acc[7]), "+v"(l), "+v"(m));
  __builtin_amdgcn_sched_barrier(0);
}

constexpr float M_NONE = -1.0e30f;

__device__ __forceinline__ u32 lds_inc(float* lds, int idx) {
  return __hip_atomic_fetch_add(&reinterpret_cast<u32*>(lds)[idx], 1u, __ATOMIC_RELAXED, __HIP_MEMORY_SCOPE_WORKGROUP);
}

__device__ __forceinline__ void layer_norm(const u32x4 (&xr)[2], float* lds, int lane) {
#pragma unroll
  for (int bb = 0; bb < 2; ++bb) {
    float xv[8];
#pragma unroll
    for (int e = 0; e < 4; ++e) { xv[2 * e] = lo16(xr[bb][e]); xv[2 * e + 1] = hi16(xr[bb][e]); }
    float s = 0.f;
#pragma unroll
    for (int e = 0; e < 8; ++e) s += xv[e];
    const float mean = wave_sum(s) * (1.f / DM);
    float s2 = 0.f;
#pragma unroll
    for (int e = 0; e < 8; ++e) { const float d = xv[e] - mean; s2 = fmaf(d, d, s2); }
    const float var = bf16r(wave_sum(s2) * (1.f / DM));
    const float mb = bf16r(mean);
    const float den = bf16r(sqrtf(bf16r(var + 1e-5f)));
    f32x4 o0, o1;
#pragma unroll
    for (int e = 0; e < 4; ++e) { o0[e] = bf16r(bf16r(xv[e] - mb) / den); o1[e] = bf16r(bf16r(xv[4 + e] - mb) / den); }
    *reinterpret_cast<f32x4*>(&lds[L_XN + bb * DM + lane * 8]) = o0;
    *reinterpret_cast<f32x4*>(&lds[L_XN + bb * DM + lane * 8 + 4]) = o1;
  }
  asm volatile("s_waitcnt lgkmcnt(0)" ::: "memory");
}

__global__ void __launch_bounds__(NTHR)
fused_decode_kernel(const unsigned short* __restrict__ x, const char* __restrict__ kc, const char* __restrict__ vc,
                    const char* __restrict__ wq, const char* __restrict__ wo, const char* __restrict__ wg,
                    const char* __restrict__ wu, const char* __restrict__ wd, unsigned short* __restrict__ out,
                    char* __restrict__ ws) {
  __shared__ __attribute__((aligned(16))) float lds[L_END];
  const int tid = threadIdx.x, lane = tid & 63, wave = __builtin_amdgcn_readfirstlane(tid >> 6);
  const int r = lane >> 4, c = lane & 15;
  const int id = blockIdx.x, i = id & 7, h = id >> 3;
  int slice = -1;
  if (i == ((h >> 1) & 7)) slice = h;
  else if (h % 3 == 0 && i == (((h / 6) * 3 + 5) & 7)) slice = 32 + h / 3;
  const int dd = ((h >> 1) - i) & 7;
  const bool is_q = (dd & 3) == 2;
  const bool ffn_wave = slice >= 0 && wave >= 1 && wave <= 4;
  const bool is_red = (i & 3) == (h & 3);
  const int red = 2 * h + (i >> 2);
  const u32 voff = lane * 16;
  const u32 tag = *(const u32*)(ws + WS_EPOCH) + 1u;
  const u32 ctag = (tag ^ 0x80000u) & 0xfffffu;
  const char* cnt = ws + WS_CNT + h * 256;
  STAMPW(0, 0);
  if (tid < 4) reinterpret_cast<u32*>(lds)[L_CNT + tid] = 0u;
  __syncthreads();

  if (i == 0 && tid == 0)
    __hip_atomic_store((__attribute__((address_space(1))) u32*)(ws + WS_CNT + h * 256), (ctag << 12) | (u32)(8 * NW * NSTAT),
                       __ATOMIC_RELAXED, __HIP_MEMORY_SCOPE_AGENT);
  u32x2 wor[4];
  auto load_wo = [&]() {
#pragma unroll
    for (int k = 0; k < 4; ++k) ld_nt2(wor[k], (u32)((4 * r + k) * (DM * 2) + c * 8), wo + (size_t)(h * DH + 16 * i) * (DM * 2) + wave * 128);
  };
  load_wo();
  if (!is_q && !ffn_wave) {
    const u64 t_start = __builtin_amdgcn_s_memrealtime();
    const size_t hb = (size_t)h * SK * DH * 2 + (size_t)(i * NW + wave) * (UKEYS * DH * 2);
    u32 t0, t1, t2;
    asm volatile("global_load_dword %0, %3, %4 nt\n\tglobal_load_dword %1, %3, %5 nt\n\tglobal_load_dword %2, %3, %6 sc1"
                 : "=&v"(t0), "=&v"(t1), "=&v"(t2) : "v"(0u), "s"(kc + hb), "s"(vc + hb), "s"(ws + WS_Q + h * 512) : "memory");
    for (int spins = 0; spins < 4096 && __builtin_amdgcn_s_memrealtime() - t_start < HOLD; ++spins) __builtin_amdgcn_s_sleep(2);
    asm volatile("s_waitcnt vmcnt(0)" : "+v"(t0), "+v"(t1), "+v"(t2) :: "memory");
  }
  if (is_q) {
    const int qi = 2 * h + (dd >> 2);
    u32x4 xr[2], wr[8];
    ld_plain(xr[0], voff, (const char*)x);
    ld_plain(xr[1], voff, (const char*)x + 1024);
    {
      const u32 vo = (lane >> 3) * (NH * DH * 2) + (lane & 7) * 16;
#pragma unroll
      for (int k = 0; k < 8; ++k) LD_WQ(wr[k], vo, wq + qi * 128 + (size_t)(64 * wave + 8 * k) * (NH * DH * 2));
    }
    wait_1<8>(xr[0]); wait_1<8>(xr[1]);
    layer_norm(xr, lds, lane);
    float qa[2][8];
#pragma unroll
    for (int e = 0; e < 8; ++e) { qa[0][e] = 0.f; qa[1][e] = 0.f; }
    for (int z = 0; z < 8; ++z) wait_1<0>(wr[z]);
#pragma unroll
    for (int k = 0; k < 8; ++k) {
      const float x0 = lds[L_XN + 64 * wave + (lane >> 3) + 8 * k], x1 = lds[L_XN + DM + 64 * wave + (lane >> 3) + 8 * k];
#pragma unroll
      for (int e = 0; e < 4; ++e) {
        const float a = lo16(wr[k][e]), bq = hi16(wr[k][e]);
        qa[0][2 * e] = fmaf(x0, a, qa[0][2 * e]); qa[0][2 * e + 1] = fmaf(x0, bq, qa[0][2 * e + 1]);
        qa[1][2 * e] = fmaf(x1, a, qa[1][2 * e]); qa[1][2 * e + 1] = fmaf(x1, bq, qa[1][2 * e + 1]);
      }
    }
#pragma unroll
    for (int bb = 0; bb < 2; ++bb) {
#pragma unroll
      for (int e = 0; e < 8; ++e) { const float t = qa[bb][e] + dpp<XOR8>(qa[bb][e]); qa[bb][e] = xrow_sum(t); }
      if (lane < 8) {
        float* rec = &lds[L_BIG + (wave * 2 + bb) * 64 + lane * 8];
        *reinterpret_cast<f32x4*>(rec) = f32x4{qa[bb][0], qa[bb][1], qa[bb][2], qa[bb][3]};
        *reinterpret_cast<f32x4*>(rec + 4) = f32x4{qa[bb][4], qa[bb][5], qa[bb][6], qa[bb][7]};
      }
    }
    asm volatile("s_waitcnt lgkmcnt(0)" ::: "memory");
    u32 arr = 0;
    if (lane == 0) arr = lds_inc(lds, L_CNT + 3);
    if (__builtin_amdgcn_readfirstlane(arr) == NW - 1) {
      asm volatile("" ::: "memory");
      if (lane < 32) {
        f32x4 t = {0.f, 0.f, 0.f, 0.f};
#pragma unroll
        for (int w = 0; w < NW; ++w) t += *reinterpret_cast<const f32x4*>(&lds[L_BIG + w * 128 + lane * 4]);
        const bf16x2 p0 = {(__bf16)t[0], (__bf16)t[1]}, p1 = {(__bf16)t[2], (__bf16)t[3]};
        st_dev((u32)(((lane >> 4) * NH * 64 + qi * 32 + (lane & 15) * 2) * 8), ws + WS_Q,
               __builtin_bit_cast(float, p0), __builtin_bit_cast(float, p1), tag);
      }
      STAMPW(7, wave);
    }
  } else if (ffn_wave) {
    const int fw = wave - 1, ft = tid - 64;
    u32x4 xr[2], gw[8], uw[8], dw[8];
    ld_plain(xr[0], voff, (const char*)x);
    ld_plain(xr[1], voff, (const char*)x + 1024);
    {
      const u32 vo = (u32)((ft >> 2) * (FF * 2) + (ft & 3) * 16);
#pragma unroll
      for (int k = 0; k < 8; ++k) ld_nt(gw[k], vo, wg + slice * (FS * 2) + (size_t)k * 64 * (FF * 2));
#pragma unroll
      for (int k = 0; k < 8; ++k) ld_nt(uw[k], vo, wu + slice * (FS * 2) + (size_t)k * 64 * (FF * 2));
#pragma unroll
      for (int k = 0; k < 8; ++k) ld_nt(dw[k], voff, wd + (size_t)(slice * FS + 8 * fw + k) * (DM * 2));
    }
    wait_1<24>(xr[0]); wait_1<24>(xr[1]);
    layer_norm(xr, lds, lane);
    for (int z = 0; z < 8; ++z) { wait_1<8>(gw[z]); wait_1<8>(uw[z]); }
#pragma unroll
    for (int bb = 0; bb < 2; ++bb) {
      float ga[8], ua[8];
#pragma unroll
      for (int e = 0; e < 8; ++e) { ga[e] = 0.f; ua[e] = 0.f; }
#pragma unroll
      for (int k = 0; k < 8; ++k) {
        const float xv = lds[L_XN + bb * DM + (ft >> 2) + 64 * k];
#pragma unroll
        for (int e = 0; e < 4; ++e) {
          ga[2 * e] = fmaf(xv, lo16(gw[k][e]), ga[2 * e]); ga[2 * e + 1] = fmaf(xv, hi16(gw[k][e]), ga[2 * e + 1]);
          ua[2 * e] = fmaf(xv, lo16(uw[k][e]), ua[2 * e]); ua[2 * e + 1] = fmaf(xv, hi16(uw[k][e]), ua[2 * e + 1]);
        }
      }
#pragma unroll
      for (int e = 0; e < 8; ++e) { ga[e] = xrow_sum(row_sum_q4(ga[e])); ua[e] = xrow_sum(row_sum_q4(ua[e])); }
      if (lane < 4) {
        float* rec = &lds[L_FR + fw * 128 + bb * 64 + lane * 8];
        *reinterpret_cast<f32x4*>(rec) = f32x4{ga[0], ga[1], ga[2], ga[3]};
        *reinterpret_cast<f32x4*>(rec + 4) = f32x4{ga[4], ga[5], ga[6], ga[7]};
        *reinterpret_cast<f32x4*>(rec + 32) = f32x4{ua[0], ua[1], ua[2], ua[3]};
        *reinterpret_cast<f32x4*>(rec + 36) = f32x4{ua[4], ua[5], ua[6], ua[7]};
      }
    }
    asm volatile("s_waitcnt lgkmcnt(0)" ::: "memory");
    u32 arr = 0;
    if (lane == 0) arr = lds_inc(lds, L_CNT + 0);
    if (__builtin_amdgcn_readfirstlane(arr) == 3) {
      asm volatile("" ::: "memory");
      const int bb = lane >> 5, col = lane & 31;
      float g = 0.f, u = 0.f;
#pragma unroll
      for (int w = 0; w < 4; ++w) { g += lds[L_FR + w * 128 + bb * 64 + col]; u += lds[L_FR + w * 128 + bb * 64 + 32 + col]; }
      g = bf16r(g); u = bf16r(u);
      const float inner = 0.7978845608028654f * (g + 0.044715f * g * g * g);
      const float ge = bf16r(0.5f * g * (1.f + tanhf(inner)));
      lds[L_HS + lane] = bf16r(ge * u);
      asm volatile("s_waitcnt lgkmcnt(0)" ::: "memory");
      if (lane == 0) __hip_atomic_store(&reinterpret_cast<u32*>(lds)[L_CNT + 1], 1u, __ATOMIC_RELAXED, __HIP_MEMORY_SCOPE_WORKGROUP);
    }
    for (u32 spins = 0; spins < SPIN_CAP; ++spins) {
      if (__hip_atomic_load(&reinterpret_cast<u32*>(lds)[L_CNT + 1], __ATOMIC_RELAXED, __HIP_MEMORY_SCOPE_WORKGROUP) != 0u) break;
      __builtin_amdgcn_s_sleep(1);
    }
    asm volatile("" ::: "memory");
    for (int z = 0; z < 8; ++z) wait_1<0>(dw[z]);
#pragma unroll
    for (int bb = 0; bb < 2; ++bb) {
      float da[8];
#pragma unroll
      for (int e = 0; e < 8; ++e) da[e] = 0.f;
#pragma unroll
      for (int k = 0; k < 8; ++k) {
        const float hv = lds[L_HS + bb * 32 + 8 * fw + k];
#pragma unroll
        for (int e = 0; e < 4; ++e) { da[2 * e] = fmaf(hv, lo16(dw[k][e]), da[2 * e]); da[2 * e + 1] = fmaf(hv, hi16(dw[k][e]), da[2 * e + 1]); }
      }
      float* rec = &lds[L_BIG + (fw * 2 + bb) * DM + lane * 8];
      *reinterpret_cast<f32x4*>(rec) = f32x4{da[0], da[1], da[2], da[3]};
      *reinterpret_cast<f32x4*>(rec + 4) = f32x4{da[4], da[5], da[6], da[7]};
    }
    asm volatile("s_waitcnt lgkmcnt(0)" ::: "memory");
    if (lane == 0) arr = lds_inc(lds, L_CNT + 2);
    if (__builtin_amdgcn_readfirstlane(arr) == 3) {
      asm volatile("" ::: "memory");
      const int pc = lane & 7;
#pragma unroll
      for (int k = 0; k < 8; ++k) {
        const int rd = 8 * k + (lane >> 3);
        const int idx = (pc >> 2) * DM + rd * 8 + 2 * (pc & 3);
        float s0 = 0.f, s1 = 0.f;
#pragma unroll
        for (int w = 0; w < 4; ++w) { const f32x2 t2 = *reinterpret_cast<const f32x2*>(&lds[L_BIG + w * 2 * DM + idx]); s0 += t2[0]; s1 += t2[1]; }
        st_dev((u32)((rd * NSLICE + slice) * 128 + pc * 16), ws + WS_F, s0, s1, tag);
      }
    }
    STAMPW(2, 1);
  }

  constexpr size_t UB = (size_t)UKEYS * DH * 2;
  const char* kp = kc + (size_t)h * SK * DH * 2;
  const char* vp = vc + (size_t)h * SK * DH * 2;
  auto uoff = [&](int u) -> size_t {
    u = min(u, UEND - 1);
    return (size_t)(u & (UPB - 1)) * UB + (size_t)(u >> 10) * ((size_t)NH * SK * DH * 2);
  };
  Stage A, B, C, D;
  const int us = i * NW + wave;
  issue(A, voff, kp + us * UB, vp + us * UB);
  issue(B, voff, kp + (64 + us) * UB, vp + (64 + us) * UB);
  issue(C, voff, kp + (128 + us) * UB, vp + (128 + us) * UB);
  STAMPW(24 + wave, wave);
  wait_n<POLL_AT>();
  u32 tA, tB;
  u32x4 q0, q1;
  {
    u32x4 g[4];
    const u32 qo = (u32)((h * 64 + 4 * c) * 8);
    ticket(tA, cnt); ticket(tB, cnt);
    ld_dev(g[0], qo, ws + WS_Q); ld_dev(g[1], qo + 16, ws + WS_Q);
    ld_dev(g[2], qo, ws + WS_Q + NH * 64 * 8); ld_dev(g[3], qo + 16, ws + WS_Q + NH * 64 * 8);
    issue(D, voff, kp + (192 + us) * UB, vp + (192 + us) * UB);
    wait_1<8>(g[0]); wait_1<8>(g[1]); wait_1<8>(g[2]); wait_1<8>(g[3]);
    for (u32 spins = 0; spins < SPIN_CAP; ++spins) {
      bool ok = true;
#pragma unroll
      for (int k = 0; k < 4; ++k) ok = ok && g[k][1] == tag && g[k][3] == tag;
      if (__all(ok)) break;
      __builtin_amdgcn_s_sleep(2);
      ld_dev(g[0], qo, ws + WS_Q); ld_dev(g[1], qo + 16, ws + WS_Q);
      ld_dev(g[2], qo, ws + WS_Q + NH * 64 * 8); ld_dev(g[3], qo + 16, ws + WS_Q + NH * 64 * 8);
      wait_1<0>(g[0]); wait_1<0>(g[1]); wait_1<0>(g[2]); wait_1<0>(g[3]);
    }
    q0 = u32x4{g[0][0], g[0][2], g[1][0], g[1][2]};
    q1 = u32x4{g[2][0], g[2][2], g[3][0], g[3][2]};
  }
  STAMPW(8 + wave, wave);
  float m = M_NONE, l = 0.f, acc[8];
#pragma unroll
  for (int e = 0; e < 8; ++e) acc[e] = 0.f;
  auto next_unit = [&](u32 val) -> int {
    for (u32 spins = 0; (val >> 12) != ctag; ++spins) {
      if (spins >= SPIN_CAP) return UEND;
      u32 t2;
      ticket(t2, cnt); val = read_ticket<0>(t2);
    }
    return (int)min(val & 0xfffu, (u32)UEND);
  };
  auto flush = [&](int bb) {
    const float mw = xrow_max(m);
    const float sc = __builtin_amdgcn_exp2f(m - mw);
    const float lw = xrow_sum(l * sc);
    float a[8];
#pragma unroll
    for (int e = 0; e < 8; ++e) a[e] = xrow_sum(acc[e] * sc);
    if (r == 0) {
      float* rec = &lds[L_REC + (bb * NW + wave) * DH + 8 * c];
      *reinterpret_cast<f32x4*>(rec) = f32x4{a[0], a[1], a[2], a[3]};
      *reinterpret_cast<f32x4*>(rec + 4) = f32x4{a[4], a[5], a[6], a[7]};
      if (c == 0) { lds[L_MM + bb * NW + wave] = mw; lds[L_LL + bb * NW + wave] = lw; }
    }
    m = M_NONE; l = 0.f;
#pragma unroll
    for (int e = 0; e < 8; ++e) acc[e] = 0.f;
  };
  int uA, uB;
  for (int k = 0; k < 4; ++k) wait_1<8>(wor[k]);
  consume<8>(A, q0, m, l, acc); uA = next_unit(read_ticket<8>(tA)); refill(A, tA, voff, kp + uoff(uA), vp + uoff(uA), cnt, uA);
  consume<8>(B, q0, m, l, acc); uB = next_unit(read_ticket<17>(tB)); refill(B, tB, voff, kp + uoff(uB), vp + uoff(uB), cnt, uB);
  consume<8>(C, q0, m, l, acc);
  consume<18>(D, q0, m, l, acc);
  u32x4 q = q0;
  int curb = 0;
  int fails = (uA >= UEND ? 1 : 0) + (uB >= UEND ? 1 : 0);
  while (fails < 2) {
    if (uA < UEND) {
      if (fails) wait_n<0>();
      if (uA >= UPB && curb == 0) { flush(0); q = q1; curb = 1; }
      consume<10>(A, q, m, l, acc);
      uA = next_unit(read_ticket<9>(tA)); refill(A, tA, voff, kp + uoff(uA), vp + uoff(uA), cnt, uA);
      STAMPU(uA);
      if (uA >= UEND) ++fails;
    }
    if (uB < UEND) {
      if (fails) wait_n<0>();
      if (uB >= UPB && curb == 0) { flush(0); q = q1; curb = 1; }
      consume<10>(B, q, m, l, acc);
      uB = next_unit(read_ticket<9>(tB)); refill(B, tB, voff, kp + uoff(uB), vp + uoff(uB), cnt, uB);
      STAMPU(uB);
      if (uB >= UEND) ++fails;
    }
  }
  STAMPW(16 + wave, wave);
  if (curb == 0) flush(0);
  flush(1);
  __syncthreads();

  if (wave < 2) {
    const int bb = 1 - wave;
    const f32x4 m0 = *reinterpret_cast<const f32x4*>(&lds[L_MM + bb * NW]), m1 = *reinterpret_cast<const f32x4*>(&lds[L_MM + bb * NW + 4]);
    const f32x4 l0 = *reinterpret_cast<const f32x4*>(&lds[L_LL + bb * NW]), l1 = *reinterpret_cast<const f32x4*>(&lds[L_LL + bb * NW + 4]);
    const float M = fmaxf(fmaxf(fmaxf(m0[0], m0[1]), fmaxf(m0[2], m0[3])), fmaxf(fmaxf(m1[0], m1[1]), fmaxf(m1[2], m1[3])));
    float L = 0.f, a0 = 0.f, a1 = 0.f;
#pragma unroll
    for (int w = 0; w < NW; ++w) {
      const float wi = __builtin_amdgcn_exp2f((w < 4 ? m0[w & 3] : m1[w & 3]) - M);
      L = fmaf(wi, w < 4 ? l0[w & 3] : l1[w & 3], L);
      const f32x2 a2 = *reinterpret_cast<const f32x2*>(&lds[L_REC + (bb * NW + w) * DH + 2 * lane]);
      a0 = fmaf(wi, a2[0], a0); a1 = fmaf(wi, a2[1], a1);
    }
    st_dev((u32)(((((lane >> 3) * 2 + bb) * 8 + i) * 16 + 2 * (lane & 7)) * 8), ws + WS_P + (size_t)h * (8 * 2 * 8 * 128), a0, a1, tag);
    if (lane == 0) st_dev((u32)(((h * 2 + bb) * 8 + i) * 16), ws + WS_ML, M, L, tag);
  }
  STAMPW(4, 0);
  if (wave == 0) {
    const int bb = (lane >> 3) & 1, j = lane & 7;
    const u32 po = (u32)(((bb * 8 + j) * 16 + 4 * r) * 8), mo = (u32)(((h * 2 + bb) * 8 + j) * 16);
    const char* pb = ws + WS_P + (size_t)(h * 8 + i) * (2 * 8 * 128);
    u32x4 g0, g1, gm;
    for (u32 spins = 0; spins < SPIN_CAP; ++spins) {
      ld_dev(g0, po, pb); ld_dev(g1, po + 16, pb); ld_dev(gm, mo, ws + WS_ML);
      wait_1<0>(g0); wait_1<0>(g1); wait_1<0>(gm);
      const bool ok = g0[1] == tag && g0[3] == tag && g1[1] == tag && g1[3] == tag && gm[1] == tag && gm[3] == tag;
      if (__all(ok)) break;
      __builtin_amdgcn_s_sleep(1);
    }
    const float mj = __uint_as_float(gm[0]), lj = __uint_as_float(gm[2]);
    float Mh = fmaxf(mj, dpp<XOR1>(mj)); Mh = fmaxf(Mh, dpp<XOR2>(Mh)); Mh = fmaxf(Mh, dpp<XOR7>(Mh));
    const float wj = __builtin_amdgcn_exp2f(mj - Mh);
    float v[5] = {wj * lj, wj * __uint_as_float(g0[0]), wj * __uint_as_float(g0[2]), wj * __uint_as_float(g1[0]), wj * __uint_as_float(g1[2])};
#pragma unroll
    for (int e = 0; e < 5; ++e) { v[e] += dpp<XOR1>(v[e]); v[e] += dpp<XOR2>(v[e]); v[e] += dpp<XOR7>(v[e]); }
    if (j == 0) *reinterpret_cast<f32x4*>(&lds[L_O + bb * 16 + 4 * r]) = f32x4{bf16r(v[1] / v[0]), bf16r(v[2] / v[0]), bf16r(v[3] / v[0]), bf16r(v[4] / v[0])};
  } else if (is_red && wave >= 2) {
    const int e = tid - 128;
    if (e < NSLICE * 8) {
      u32x4 g;
      for (u32 spins = 0; spins < SPIN_CAP; ++spins) {
        ld_dev(g, (u32)(e * 16), ws + WS_F + (size_t)red * (NSLICE * 128));
        wait_1<0>(g);
        if (__all(g[1] == tag && g[3] == tag)) break;
        __builtin_amdgcn_s_sleep(4);
      }
      *reinterpret_cast<f32x2*>(&lds[L_BIG + 2 * e]) = f32x2{__uint_as_float(g[0]), __uint_as_float(g[2])};
    }
  }
  __syncthreads();
  STAMPW(5, 0);
  {
    const f32x4 o0 = *reinterpret_cast<const f32x4*>(&lds[L_O + 4 * r]), o1 = *reinterpret_cast<const f32x4*>(&lds[L_O + 16 + 4 * r]);
    float t0[4], t1[4];
#pragma unroll
    for (int e = 0; e < 4; ++e) { t0[e] = 0.f; t1[e] = 0.f; }
#pragma unroll
    for (int k = 0; k < 4; ++k) {
      const float w4[4] = {lo16(wor[k][0]), hi16(wor[k][0]), lo16(wor[k][1]), hi16(wor[k][1])};
#pragma unroll
      for (int e = 0; e < 4; ++e) { t0[e] = fmaf(o0[k], w4[e], t0[e]); t1[e] = fmaf(o1[k], w4[e], t1[e]); }
    }
#pragma unroll
    for (int e = 0; e < 4; ++e) { t0[e] = xrow_sum(t0[e]); t1[e] = xrow_sum(t1[e]); }
    const float sa = r == 0 ? t0[0] : r == 1 ? t0[2] : r == 2 ? t1[0] : t1[2];
    const float sb = r == 0 ? t0[1] : r == 1 ? t0[3] : r == 2 ? t1[1] : t1[3];
    st_dev((u32)(((8 * wave + (c >> 1)) * NWG + id) * 128 + (r >> 1) * 64 + ((c & 1) * 4 + (r & 1) * 2) * 8), ws + WS_T, sa, sb, tag);
  }
  if (!is_red) return;

  float gate = 0.f;
  if (tid < 16) {
    float d = 0.f;
#pragma unroll
    for (int s = 0; s < NSLICE; ++s) d += lds[L_BIG + s * 16 + tid];
    d = bf16r(d);
    gate = bf16r(d / (1.f + __expf(-d)));
  }
  {
    const char* tb = ws + WS_T + (size_t)red * (NWG * 128);
    u32x4 g[4];
    for (u32 spins = 0; spins < SPIN_CAP; ++spins) {
#pragma unroll
      for (int k = 0; k < 4; ++k) ld_dev(g[k], (u32)(tid * 16), tb + k * 8192);
      bool ok = true;
#pragma unroll
      for (int k = 0; k < 4; ++k) { wait_1<0>(g[k]); ok = ok && g[k][1] == tag && g[k][3] == tag; }
      if (__all(ok)) break;
      __builtin_amdgcn_s_sleep(1);
    }
    float s0 = 0.f, s1 = 0.f;
#pragma unroll
    for (int k = 0; k < 4; ++k) { s0 += __uint_as_float(g[k][0]); s1 += __uint_as_float(g[k][2]); }
    s0 += dpp<XOR8>(s0); s1 += dpp<XOR8>(s1);
    s0 = xrow_sum(s0); s1 = xrow_sum(s1);
    if (lane < 8) *reinterpret_cast<f32x2*>(&lds[L_FR + wave * 16 + 2 * lane]) = f32x2{s0, s1};
  }
  __syncthreads();
  if (tid < 16) {
    float s = 0.f;
#pragma unroll
    for (int w = 0; w < NW; ++w) s += lds[L_FR + w * 16 + tid];
    const int o = (tid >> 3) * DM + red * 8 + (tid & 7);
    const float xv = __uint_as_float((u32)x[o] << 16);
    out[o] = __builtin_bit_cast(unsigned short, (__bf16)(xv + bf16r(bf16r(s) * gate)));
    if (red == 0 && tid == 0) *(u32*)(ws + WS_EPOCH) = tag;
    STAMPW(6, 0);
  }
}

extern "C" void kernel_launch(void* const* d_in, const int* in_sizes, int n_in, void* d_out, int out_size, void* d_ws,
                              size_t ws_size, hipStream_t stream) {
  (void)in_sizes; (void)n_in; (void)out_size;
  if (ws_size < WS_END) return;
  fused_decode_kernel<<<NWG, NTHR, 0, stream>>>((const unsigned short*)d_in[0], (const char*)d_in[1], (const char*)d_in[2],
                                                (const char*)d_in[3], (const char*)d_in[4], (const char*)d_in[6],
                                                (const char*)d_in[7], (const char*)d_in[8], (unsigned short*)d_out, (char*)d_ws);
}
```

```cpp
#include <hip/hip_runtime.h>
#include <stdint.h>

typedef unsigned u32;
typedef unsigned long long u64;
typedef u32 u32x4 __attribute__((ext_vector_type(4)));
typedef u32 u32x2 __attribute__((ext_vector_type(2)));
typedef float f32x4 __attribute__((ext_vector_type(4)));
typedef float f32x2 __attribute__((ext_vector_type(2)));
typedef __bf16 bf16x2 __attribute__((ext_vector_type(2)));

constexpr int DM = 512, NH = 32, SK = 16384, DH = 128, FF = 1376;
constexpr int NW = 8, NTHR = NW * 64, NWG = 256;
constexpr int UKEYS = 16;
constexpr int UPB = SK / UKEYS;
constexpr int UEND = 2 * UPB;
constexpr int NSTAT = 4;
constexpr int NSLICE = 43, FS = 32;
constexpr int NRED = 64;
constexpr u32 SPIN_CAP = 1u << 20;
#define LD_WQ ld_nt
#define HOLD 100
#define POLL_AT 8

constexpr size_t WS_EPOCH = 0;
#define CNT_OFF 8388608
#define CNT_STRIDE 256
constexpr size_t WS_CNT = CNT_OFF;
constexpr size_t WS_Q = 16384;
constexpr size_t WS_ML = WS_Q + 2 * NH * 64 * 8;
constexpr size_t WS_P = WS_ML + NH * 2 * 8 * 16;
constexpr size_t WS_T = WS_P + (size_t)NH * 8 * 2 * 8 * 128;
constexpr size_t WS_F = WS_T + (size_t)NRED * NWG * 128;
constexpr size_t WS_END = WS_F + (size_t)NRED * NSLICE * 128;

constexpr int L_XN = 0;
constexpr int L_REC = L_XN + 2 * DM;
constexpr int L_MM = L_REC + 2 * NW * DH;
constexpr int L_LL = L_MM + 16;
constexpr int L_O = L_LL + 16;
constexpr int L_HS = L_O + 32;
constexpr int L_CNT = L_HS + 64;
constexpr int L_FR = L_CNT + 4;
constexpr int L_BIG = L_FR + 512;
constexpr int L_END = L_BIG + 4 * 2 * DM;

__device__ __forceinline__ float bf16r(float x) { return (float)(__bf16)x; }
__device__ __forceinline__ float lo16(u32 w) { return __uint_as_float(w << 16); }
__device__ __forceinline__ float hi16(u32 w) { return __uint_as_float(w & 0xffff0000u); }
__device__ __forceinline__ float dot2(u32 a, u32 b, float c) {
  return __builtin_amdgcn_fdot2_f32_bf16(__builtin_bit_cast(bf16x2, a), __builtin_bit_cast(bf16x2, b), c, false);
}
template <int CTRL> __device__ __forceinline__ float dpp(float x) {
  return __builtin_bit_cast(float, __builtin_amdgcn_mov_dpp(__builtin_bit_cast(int, x), CTRL, 0xf, 0xf, true));
}
constexpr int XOR1 = 0xB1, XOR2 = 0x4E, XOR7 = 0x141, XOR8 = 0x128, ROR4 = 0x124;
__device__ __forceinline__ float row_sum16(float x) {
  x += dpp<XOR1>(x); x += dpp<XOR2>(x); x += dpp<XOR7>(x); x += dpp<XOR8>(x); return x;
}
__device__ __forceinline__ float row_sum_q4(float x) {
  x += dpp<ROR4>(x); x += dpp<XOR8>(x); return x;
}
__device__ __forceinline__ float xrow_max(float x) {
  auto s = __builtin_amdgcn_permlane16_swap(__float_as_uint(x), __float_as_uint(x), false, false);
  x = fmaxf(__uint_as_float(s[0]), __uint_as_float(s[1]));
  auto t = __builtin_amdgcn_permlane32_swap(__float_as_uint(x), __float_as_uint(x), false, false);
  return fmaxf(__uint_as_float(t[0]), __uint_as_float(t[1]));
}
__device__ __forceinline__ float xrow_sum(float x) {
  auto s = __builtin_amdgcn_permlane16_swap(__float_as_uint(x), __float_as_uint(x), false, false);
  x = __uint_as_float(s[0]) + __uint_as_float(s[1]);
  auto t = __builtin_amdgcn_permlane32_swap(__float_as_uint(x), __float_as_uint(x), false, false);
  return __uint_as_float(t[0]) + __uint_as_float(t[1]);
}
__device__ __forceinline__ float wave_sum(float x) { return xrow_sum(row_sum16(x)); }

#define STAMPW(i, w) do {} while (0)
#define STAMPU(u) do {} while (0)

struct Stage { u32x4 k[4]; u32x4 v[4]; };

#define STAGE_REGS(s) "+v"(s.k[0]), "+v"(s.k[1]), "+v"(s.k[2]), "+v"(s.k[3]), "+v"(s.v[0]), "+v"(s.v[1]), "+v"(s.v[2]), "+v"(s.v[3])
__device__ __forceinline__ void ld_plain(u32x4& d, u32 voff, const char* base) {
  asm volatile("s_nop 4\n\tglobal_load_dwordx4 %0, %1, %2" : "=&v"(d) : "v"(voff), "s"(base) : "memory");
}
__device__ __forceinline__ void ld_nt2(u32x2& d, u32 voff, const char* base) {
  asm volatile("s_nop 4\n\tglobal_load_dwordx2 %0, %1, %2 nt" : "=&v"(d) : "v"(voff), "s"(base) : "memory");
}
__device__ __forceinline__ void ld_nt(u32x4& d, u32 voff, const char* base) {
  asm volatile("s_nop 4\n\tglobal_load_dwordx4 %0, %1, %2 nt" : "=&v"(d) : "v"(voff), "s"(base) : "memory");
}
__device__ __forceinline__ void ld_dev(u32x4& d, u32 voff, const char* base) {
  asm volatile("s_nop 4\n\tglobal_load_dwordx4 %0, %1, %2 sc1" : "=&v"(d) : "v"(voff), "s"(base) : "memory");
}
__device__ __forceinline__ void st_dev(u32 voff, char* base, float a, float b, u32 tag) {
  const u32x4 d = {__float_as_uint(a), tag, __float_as_uint(b), tag};
  asm volatile("s_nop 4\n\tglobal_store_dwordx4 %0, %1, %2 sc1" :: "v"(voff), "v"(d), "s"(base) : "memory");
}
__device__ __forceinline__ void ticket(u32& t, const char* cnt) {
  asm volatile("s_nop 4\n\ts_mov_b64 exec, 1\n\tglobal_atomic_add %0, %1, %2, %3 sc0\n\ts_mov_b64 exec, -1"
               : "=&v"(t) : "v"(0u), "v"(1u), "s"(cnt) : "memory");
}
template <int N> __device__ __forceinline__ u32 read_ticket(u32 t) {
  u32 v;
  asm volatile("s_waitcnt vmcnt(%2)\n\tv_readfirstlane_b32 %0, %1\n\ts_nop 4" : "=s"(v) : "v"(t), "i"(N) : "memory");
  return v;
}
__device__ __forceinline__ void issue(Stage& s, u32 voff, const char* kb, const char* vb) {
  asm volatile("s_nop 4\n\t"
               "global_load_dwordx4 %0, %8, %9 nt\n\t"
               "global_load_dwordx4 %1, %8, %9 offset:1024 nt\n\t"
               "global_load_dwordx4 %2, %8, %9 offset:2048 nt\n\t"
               "global_load_dwordx4 %3, %8, %9 offset:3072 nt\n\t"
               "global_load_dwordx4 %4, %8, %10 nt\n\t"
               "global_load_dwordx4 %5, %8, %10 offset:1024 nt\n\t"
               "global_load_dwordx4 %6, %8, %10 offset:2048 nt\n\t"
               "global_load_dwordx4 %7, %8, %10 offset:3072 nt"
               : "=&v"(s.k[0]), "=&v"(s.k[1]), "=&v"(s.k[2]), "=&v"(s.k[3]), "=&v"(s.v[0]), "=&v"(s.v[1]), "=&v"(s.v[2]), "=&v"(s.v[3])
               : "v"(voff), "s"(kb), "s"(vb) : "memory");
}
__device__ __forceinline__ void refill(Stage& s, u32& t, u32 voff, const char* kb, const char* vb, const char* cnt, int u) {
  asm volatile("s_nop 4\n\t"
               "s_cmpk_lt_i32 %15, 0x800\n\t"
               "s_cbranch_scc0 1f\n\t"
               "global_load_dwordx4 %0, %9, %10 nt\n\t"
               "global_load_dwordx4 %1, %9, %10 offset:1024 nt\n\t"
               "global_load_dwordx4 %2, %9, %10 offset:2048 nt\n\t"
               "global_load_dwordx4 %3, %9, %10 offset:3072 nt\n\t"
               "global_load_dwordx4 %4, %9, %11 nt\n\t"
               "global_load_dwordx4 %5, %9, %11 offset:1024 nt\n\t"
               "global_load_dwordx4 %6, %9, %11 offset:2048 nt\n\t"
               "global_load_dwordx4 %7, %9, %11 offset:3072 nt\n\t"
               "s_mov_b64 exec, 1\n\t"
               "global_atomic_add %8, %13, %14, %12 sc0\n\t"
               "s_mov_b64 exec, -1\n\t"
               "s_branch 2f\n"
               "1:\n\t"
               "s_waitcnt vmcnt(0)\n"
               "2:"
               : STAGE_REGS(s), "=&v"(t) : "v"(voff), "s"(kb), "s"(vb), "s"(cnt), "v"(0u), "v"(1u), "s"(u) : "memory", "scc");
}
static_assert(UEND == 0x800, "refill compares against 0x800");
template <int N> __device__ __forceinline__ void wait_k(Stage& s) {
  asm volatile("s_waitcnt vmcnt(%4)" : "+v"(s.k[0]), "+v"(s.k[1]), "+v"(s.k[2]), "+v"(s.k[3]) : "i"(N) : "memory");
}
template <int N> __device__ __forceinline__ void wait_v(Stage& s) {
  asm volatile("s_waitcnt vmcnt(%4)" : "+v"(s.v[0]), "+v"(s.v[1]), "+v"(s.v[2]), "+v"(s.v[3]) : "i"(N) : "memory");
}
template <int N, typename V> __device__ __forceinline__ void wait_1(V& d) { asm volatile("s_waitcnt vmcnt(%1)" : "+v"(d) : "i"(N) : "memory"); }
template <int N> __device__ __forceinline__ void wait_n() { asm volatile("s_waitcnt vmcnt(%0)" :: "i"(N) : "memory"); }

template <int LATER>
__device__ __forceinline__ void consume(Stage& s, const u32x4 q, float& m, float& l, float (&acc)[8]) {
  constexpr float SCALE = 0.08838834764831845f, LOG2E = 1.4426950408889634f;
  float y[4];
  wait_k<LATER + 4>(s);
#pragma unroll
  for (int t = 0; t < 4; ++t) {
    float d = dot2(s.k[t][0], q[0], 0.f);
    d = dot2(s.k[t][1], q[1], d); d = dot2(s.k[t][2], q[2], d); d = dot2(s.k[t][3], q[3], d);
    d = row_sum16(d);
    y[t] = bf16r(bf16r(d) * SCALE) * LOG2E;
  }
  float mn = m;
#pragma unroll
  for (int t = 0; t < 4; ++t) mn = fmaxf(mn, y[t]);
  const float alpha = __builtin_amdgcn_exp2f(m - mn);
  float ps = 0.f;
#pragma unroll
  for (int e = 0; e < 8; ++e) acc[e] *= alpha;
  wait_v<LATER>(s);
#pragma unroll
  for (int t = 0; t < 4; ++t) {
    const float p = __builtin_amdgcn_exp2f(y[t] - mn);
    ps += p;
#pragma unroll
    for (int e = 0; e < 4; ++e) {
      acc[2 * e] = fmaf(p, lo16(s.v[t][e]), acc[2 * e]);
      acc[2 * e + 1] = fmaf(p, hi16(s.v[t][e]), acc[2 * e + 1]);
    }
  }
  l = fmaf(l, alpha, ps);
  m = mn;
  asm volatile("" : "+v"(acc[0]), "+v"(acc[1]), "+v"(acc[2]), "+v"(acc[3]), "+v"(acc[4]), "+v"(acc[5]), "+v"(acc[6]), "+v"(acc[7]), "+v"(l), "+v"(m));
  __builtin_amdgcn_sched_barrier(0);
}

constexpr float M_NONE = -1.0e30f;

__device__ __forceinline__ u32 lds_inc(float* lds, int idx) {
  return __hip_atomic_fetch_add(&reinterpret_cast<u32*>(lds)[idx], 1u, __ATOMIC_RELAXED, __HIP_MEMORY_SCOPE_WORKGROUP);
}

__device__ __forceinline__ void layer_norm(const u32x4 (&xr)[2], float* lds, int lane) {
#pragma unroll
  for (int bb = 0; bb < 2; ++bb) {
    float xv[8];
#pragma unroll
    for (int e = 0; e < 4; ++e) { xv[2 * e] = lo16(xr[bb][e]); xv[2 * e + 1] = hi16(xr[bb][e]); }
    float s = 0.f;
#pragma unroll
    for (int e = 0; e < 8; ++e) s += xv[e];
    const float mean = wave_sum(s) * (1.f / DM);
    float s2 = 0.f;
#pragma unroll
    for (int e = 0; e < 8; ++e) { const float d = xv[e] - mean; s2 = fmaf(d, d, s2); }
    const float var = bf16r(wave_sum(s2) * (1.f / DM));
    const float mb = bf16r(mean);
    const float den = bf16r(sqrtf(bf16r(var + 1e-5f)));
    f32x4 o0, o1;
#pragma unroll
    for (int e = 0; e < 4; ++e) { o0[e] = bf16r(bf16r(xv[e] - mb) / den); o1[e] = bf16r(bf16r(xv[4 + e] - mb) / den); }
    *reinterpret_cast<f32x4*>(&lds[L_XN + bb * DM + lane * 8]) = o0;
    *reinterpret_cast<f32x4*>(&lds[L_XN + bb * DM + lane * 8 + 4]) = o1;
  }
  asm volatile("s_waitcnt lgkmcnt(0)" ::: "memory");
}

__global__ void __launch_bounds__(NTHR)
fused_decode_kernel(const unsigned short* __restrict__ x, const char* __restrict__ kc, const char* __restrict__ vc,
                    const char* __restrict__ wq, const char* __restrict__ wo, const char* __restrict__ wg,
                    const char* __restrict__ wu, const char* __restrict__ wd, unsigned short* __restrict__ out,
                    char* __restrict__ ws) {
  __shared__ __attribute__((aligned(16))) float lds[L_END];
  const int tid = threadIdx.x, lane = tid & 63, wave = __builtin_amdgcn_readfirstlane(tid >> 6);
  const int r = lane >> 4, c = lane & 15;
  const int id = blockIdx.x, i = id & 7, h = id >> 3;
  int slice = -1;
  if (i == ((h >> 1) & 7)) slice = h;
  else if (h % 3 == 0 && i == (((h / 6) * 3 + 5) & 7)) slice = 32 + h / 3;
  const int dd = ((h >> 1) - i) & 7;
  const bool is_q = (dd & 3) == 2;
  const bool ffn_wave = slice >= 0 && wave >= 1 && wave <= 4;
  const bool is_red = (i & 3) == (h & 3);
  const int red = 2 * h + (i >> 2);
  const u32 voff = lane * 16;
  const u32 tag = *(const u32*)(ws + WS_EPOCH) + 1u;
  const u32 ctag = (tag ^ 0x80000u) & 0xfffffu;
  const char* cnt = ws + WS_CNT + (size_t)h * CNT_STRIDE;
  STAMPW(0, 0);
  if (tid < 4) reinterpret_cast<u32*>(lds)[L_CNT + tid] = 0u;
  __syncthreads();

  if (i == 0 && tid == 0)
    __hip_atomic_store((__attribute__((address_space(1))) u32*)(ws + WS_CNT + (size_t)h * CNT_STRIDE), (ctag << 12) | (u32)(8 * NW * NSTAT),
                       __ATOMIC_RELAXED, __HIP_MEMORY_SCOPE_AGENT);
  u32x2 wor[4];
  auto load_wo = [&]() {
#pragma unroll
    for (int k = 0; k < 4; ++k) ld_nt2(wor[k], (u32)((4 * r + k) * (DM * 2) + c * 8), wo + (size_t)(h * DH + 16 * i) * (DM * 2) + wave * 128);
  };
  load_wo();
  if (!is_q && !ffn_wave) {
    const u64 t_start = __builtin_amdgcn_s_memrealtime();
    const size_t hb = (size_t)h * SK * DH * 2 + (size_t)(i * NW + wave) * (UKEYS * DH * 2);
    u32 t0, t1, t2;
    asm volatile("global_load_dword %0, %3, %4 nt\n\tglobal_load_dword %1, %3, %5 nt\n\tglobal_load_dword %2, %3, %6 sc1"
                 : "=&v"(t0), "=&v"(t1), "=&v"(t2) : "v"(0u), "s"(kc + hb), "s"(vc + hb), "s"(ws + WS_Q + h * 512) : "memory");
    for (int spins = 0; spins < 4096 && __builtin_amdgcn_s_memrealtime() - t_start < HOLD; ++spins) __builtin_amdgcn_s_sleep(2);
    asm volatile("s_waitcnt vmcnt(0)" : "+v"(t0), "+v"(t1), "+v"(t2) :: "memory");
  }
  if (is_q) {
    const int qi = 2 * h + (dd >> 2);
    u32x4 xr[2], wr[8];
    ld_plain(xr[0], voff, (const char*)x);
    ld_plain(xr[1], voff, (const char*)x + 1024);
    {
      const u32 vo = (lane >> 3) * (NH * DH * 2) + (lane & 7) * 16;
#pragma unroll
      for (int k = 0; k < 8; ++k) LD_WQ(wr[k], vo, wq + qi * 128 + (size_t)(64 * wave + 8 * k) * (NH * DH * 2));
    }
    wait_1<8>(xr[0]); wait_1<8>(xr[1]);
    layer_norm(xr, lds, lane);
    float qa[2][8];
#pragma unroll
    for (int e = 0; e < 8; ++e) { qa[0][e] = 0.f; qa[1][e] = 0.f; }
    for (int z = 0; z < 8; ++z) wait_1<0>(wr[z]);
#pragma unroll
    for (int k = 0; k < 8; ++k) {
      const float x0 = lds[L_XN + 64 * wave + (lane >> 3) + 8 * k], x1 = lds[L_XN + DM + 64 * wave + (lane >> 3) + 8 * k];
#pragma unroll
      for (int e = 0; e < 4; ++e) {
        const float a = lo16(wr[k][e]), bq = hi16(wr[k][e]);
        qa[0][2 * e] = fmaf(x0, a, qa[0][2 * e]); qa[0][2 * e + 1] = fmaf(x0, bq, qa[0][2 * e + 1]);
        qa[1][2 * e] = fmaf(x1, a, qa[1][2 * e]); qa[1][2 * e + 1] = fmaf(x1, bq, qa[1][2 * e + 1]);
      }
    }
#pragma unroll
    for (int bb = 0; bb < 2; ++bb) {
#pragma unroll
      for (int e = 0; e < 8; ++e) { const float t = qa[bb][e] + dpp<XOR8>(qa[bb][e]); qa[bb][e] = xrow_sum(t); }
      if (lane < 8) {
        float* rec = &lds[L_BIG + (wave * 2 + bb) * 64 + lane * 8];
        *reinterpret_cast<f32x4*>(rec) = f32x4{qa[bb][0], qa[bb][1], qa[bb][2], qa[bb][3]};
        *reinterpret_cast<f32x4*>(rec + 4) = f32x4{qa[bb][4], qa[bb][5], qa[bb][6], qa[bb][7]};
      }
    }
    asm volatile("s_waitcnt lgkmcnt(0)" ::: "memory");
    u32 arr = 0;
    if (lane == 0) arr = lds_inc(lds, L_CNT + 3);
    if (__builtin_amdgcn_readfirstlane(arr) == NW - 1) {
      asm volatile("" ::: "memory");
      if (lane < 32) {
        f32x4 t = {0.f, 0.f, 0.f, 0.f};
#pragma unroll
        for (int w = 0; w < NW; ++w) t += *reinterpret_cast<const f32x4*>(&lds[L_BIG + w * 128 + lane * 4]);
        const bf16x2 p0 = {(__bf16)t[0], (__bf16)t[1]}, p1 = {(__bf16)t[2], (__bf16)t[3]};
        st_dev((u32)(((lane >> 4) * NH * 64 + qi * 32 + (lane & 15) * 2) * 8), ws + WS_Q,
               __builtin_bit_cast(float, p0), __builtin_bit_cast(float, p1), tag);
      }
      STAMPW(7, wave);
    }
  } else if (ffn_wave) {
    const int fw = wave - 1, ft = tid - 64;
    u32x4 xr[2], gw[8], uw[8], dw[8];
    ld_plain(xr[0], voff, (const char*)x);
    ld_plain(xr[1], voff, (const char*)x + 1024);
    {
      const u32 vo = (u32)((ft >> 2) * (FF * 2) + (ft & 3) * 16);
#pragma unroll
      for (int k = 0; k < 8; ++k) ld_nt(gw[k], vo, wg + slice * (FS * 2) + (size_t)k * 64 * (FF * 2));
#pragma unroll
      for (int k = 0; k < 8; ++k) ld_nt(uw[k], vo, wu + slice * (FS * 2) + (size_t)k * 64 * (FF * 2));
#pragma unroll
      for (int k = 0; k < 8; ++k) ld_nt(dw[k], voff, wd + (size_t)(slice * FS + 8 * fw + k) * (DM * 2));
    }
    wait_1<24>(xr[0]); wait_1<24>(xr[1]);
    layer_norm(xr, lds, lane);
    for (int z = 0; z < 8; ++z) { wait_1<8>(gw[z]); wait_1<8>(uw[z]); }
#pragma unroll
    for (int bb = 0; bb < 2; ++bb) {
      float ga[8], ua[8];
#pragma unroll
      for (int e = 0; e < 8; ++e) { ga[e] = 0.f; ua[e] = 0.f; }
#pragma unroll
      for (int k = 0; k < 8; ++k) {
        const float xv = lds[L_XN + bb * DM + (ft >> 2) + 64 * k];
#pragma unroll
        for (int e = 0; e < 4; ++e) {
          ga[2 * e] = fmaf(xv, lo16(gw[k][e]), ga[2 * e]); ga[2 * e + 1] = fmaf(xv, hi16(gw[k][e]), ga[2 * e + 1]);
          ua[2 * e] = fmaf(xv, lo16(uw[k][e]), ua[2 * e]); ua[2 * e + 1] = fmaf(xv, hi16(uw[k][e]), ua[2 * e + 1]);
        }
      }
#pragma unroll
      for (int e = 0; e < 8; ++e) { ga[e] = xrow_sum(row_sum_q4(ga[e])); ua[e] = xrow_sum(row_sum_q4(ua[e])); }
      if (lane < 4) {
        float* rec = &lds[L_FR + fw * 128 + bb * 64 + lane * 8];
        *reinterpret_cast<f32x4*>(rec) = f32x4{ga[0], ga[1], ga[2], ga[3]};
        *reinterpret_cast<f32x4*>(rec + 4) = f32x4{ga[4], ga[5], ga[6], ga[7]};
        *reinterpret_cast<f32x4*>(rec + 32) = f32x4{ua[0], ua[1], ua[2], ua[3]};
        *reinterpret_cast<f32x4*>(rec + 36) = f32x4{ua[4], ua[5], ua[6], ua[7]};
      }
    }
    asm volatile("s_waitcnt lgkmcnt(0)" ::: "memory");
    u32 arr = 0;
    if (lane == 0) arr = lds_inc(lds, L_CNT + 0);
    if (__builtin_amdgcn_readfirstlane(arr) == 3) {
      asm volatile("" ::: "memory");
      const int bb = lane >> 5, col = lane & 31;
      float g = 0.f, u = 0.f;
#pragma unroll
      for (int w = 0; w < 4; ++w) { g += lds[L_FR + w * 128 + bb * 64 + col]; u += lds[L_FR + w * 128 + bb * 64 + 32 + col]; }
      g = bf16r(g); u = bf16r(u);
      const float inner = 0.7978845608028654f * (g + 0.044715f * g * g * g);
      const float ge = bf16r(0.5f * g * (1.f + tanhf(inner)));
      lds[L_HS + lane] = bf16r(ge * u);
      asm volatile("s_waitcnt lgkmcnt(0)" ::: "memory");
      if (lane == 0) __hip_atomic_store(&reinterpret_cast<u32*>(lds)[L_CNT + 1], 1u, __ATOMIC_RELAXED, __HIP_MEMORY_SCOPE_WORKGROUP);
    }
    for (u32 spins = 0; spins < SPIN_CAP; ++spins) {
      if (__hip_atomic_load(&reinterpret_cast<u32*>(lds)[L_CNT + 1], __ATOMIC_RELAXED, __HIP_MEMORY_SCOPE_WORKGROUP) != 0u) break;
      __builtin_amdgcn_s_sleep(1);
    }
    asm volatile("" ::: "memory");
    for (int z = 0; z < 8; ++z) wait_1<0>(dw[z]);
#pragma unroll
    for (int bb = 0; bb < 2; ++bb) {
      float da[8];
#pragma unroll
      for (int e = 0; e < 8; ++e) da[e] = 0.f;
#pragma unroll
      for (int k = 0; k < 8; ++k) {
        const float hv = lds[L_HS + bb * 32 + 8 * fw + k];
#pragma unroll
        for (int e = 0; e < 4; ++e) { da[2 * e] = fmaf(hv, lo16(dw[k][e]), da[2 * e]); da[2 * e + 1] = fmaf(hv, hi16(dw[k][e]), da[2 * e + 1]); }
      }
      float* rec = &lds[L_BIG + (fw * 2 + bb) * DM + lane * 8];
      *reinterpret_cast<f32x4*>(rec) = f32x4{da[0], da[1], da[2], da[3]};
      *reinterpret_cast<f32x4*>(rec + 4) = f32x4{da[4], da[5], da[6], da[7]};
    }
    asm volatile("s_waitcnt lgkmcnt(0)" ::: "memory");
    if (lane == 0) arr = lds_inc(lds, L_CNT + 2);
    if (__builtin_amdgcn_readfirstlane(arr) == 3) {
      asm volatile("" ::: "memory");
      const int pc = lane & 7;
#pragma unroll
      for (int k = 0; k < 8; ++k) {
        const int rd = 8 * k + (lane >> 3);
        const int idx = (pc >> 2) * DM + rd * 8 + 2 * (pc & 3);
        float s0 = 0.f, s1 = 0.f;
#pragma unroll
        for (int w = 0; w < 4; ++w) { const f32x2 t2 = *reinterpret_cast<const f32x2*>(&lds[L_BIG + w * 2 * DM + idx]); s0 += t2[0]; s1 += t2[1]; }
        st_dev((u32)((rd * NSLICE + slice) * 128 + pc * 16), ws + WS_F, s0, s1, tag);
      }
    }
    STAMPW(2, 1);
  }

  constexpr size_t UB = (size_t)UKEYS * DH * 2;
  const char* kp = kc + (size_t)h * SK * DH * 2;
  const char* vp = vc + (size_t)h * SK * DH * 2;
  auto uoff = [&](int u) -> size_t {
    u = min(u, UEND - 1);
    return (size_t)(u & (UPB - 1)) * UB + (size_t)(u >> 10) * ((size_t)NH * SK * DH * 2);
  };
  Stage A, B, C, D;
  const int us = i * NW + wave;
  issue(A, voff, kp + us * UB, vp + us * UB);
  issue(B, voff, kp + (64 + us) * UB, vp + (64 + us) * UB);
  issue(C, voff, kp + (128 + us) * UB, vp + (128 + us) * UB);
  STAMPW(24 + wave, wave);
  wait_n<POLL_AT>();
  u32 tA, tB;
  u32x4 q0, q1;
  {
    u32x4 g[4];
    const u32 qo = (u32)((h * 64 + 4 * c) * 8);
    ticket(tA, cnt); ticket(tB, cnt);
    ld_dev(g[0], qo, ws + WS_Q); ld_dev(g[1], qo + 16, ws + WS_Q);
    ld_dev(g[2], qo, ws + WS_Q + NH * 64 * 8); ld_dev(g[3], qo + 16, ws + WS_Q + NH * 64 * 8);
    issue(D, voff, kp + (192 + us) * UB, vp + (192 + us) * UB);
    wait_1<8>(g[0]); wait_1<8>(g[1]); wait_1<8>(g[2]); wait_1<8>(g[3]);
    for (u32 spins = 0; spins < SPIN_CAP; ++spins) {
      bool ok = true;
#pragma unroll
      for (int k = 0; k < 4; ++k) ok = ok && g[k][1] == tag && g[k][3] == tag;
      if (__all(ok)) break;
      __builtin_amdgcn_s_sleep(2);
      ld_dev(g[0], qo, ws + WS_Q); ld_dev(g[1], qo + 16, ws + WS_Q);
      ld_dev(g[2], qo, ws + WS_Q + NH * 64 * 8); ld_dev(g[3], qo + 16, ws + WS_Q + NH * 64 * 8);
      wait_1<0>(g[0]); wait_1<0>(g[1]); wait_1<0>(g[2]); wait_1<0>(g[3]);
    }
    q0 = u32x4{g[0][0], g[0][2], g[1][0], g[1][2]};
    q1 = u32x4{g[2][0], g[2][2], g[3][0], g[3][2]};
  }
  STAMPW(8 + wave, wave);
  float m = M_NONE, l = 0.f, acc[8];
#pragma unroll
  for (int e = 0; e < 8; ++e) acc[e] = 0.f;
  auto next_unit = [&](u32 val) -> int {
    for (u32 spins = 0; (val >> 12) != ctag; ++spins) {
      if (spins >= SPIN_CAP) return UEND;
      u32 t2;
      ticket(t2, cnt); val = read_ticket<0>(t2);
    }
    return (int)min(val & 0xfffu, (u32)UEND);
  };
  auto flush = [&](int bb) {
    const float mw = xrow_max(m);
    const float sc = __builtin_amdgcn_exp2f(m - mw);
    const float lw = xrow_sum(l * sc);
    float a[8];
#pragma unroll
    for (int e = 0; e < 8; ++e) a[e] = xrow_sum(acc[e] * sc);
    if (r == 0) {
      float* rec = &lds[L_REC + (bb * NW + wave) * DH + 8 * c];
      *reinterpret_cast<f32x4*>(rec) = f32x4{a[0], a[1], a[2], a[3]};
      *reinterpret_cast<f32x4*>(rec + 4) = f32x4{a[4], a[5], a[6], a[7]};
      if (c == 0) { lds[L_MM + bb * NW + wave] = mw; lds[L_LL + bb * NW + wave] = lw; }
    }
    m = M_NONE; l = 0.f;
#pragma unroll
    for (int e = 0; e < 8; ++e) acc[e] = 0.f;
  };
  int uA, uB;
  for (int k = 0; k < 4; ++k) wait_1<8>(wor[k]);
  consume<8>(A, q0, m, l, acc); uA = next_unit(read_ticket<8>(tA)); refill(A, tA, voff, kp + uoff(uA), vp + uoff(uA), cnt, uA);
  consume<8>(B, q0, m, l, acc); uB = next_unit(read_ticket<17>(tB)); refill(B, tB, voff, kp + uoff(uB), vp + uoff(uB), cnt, uB);
  consume<8>(C, q0, m, l, acc);
  consume<18>(D, q0, m, l, acc);
  u32x4 q = q0;
  int curb = 0;
  int fails = (uA >= UEND ? 1 : 0) + (uB >= UEND ? 1 : 0);
  while (fails < 2) {
    if (uA < UEND) {
      if (fails) wait_n<0>();
      if (uA >= UPB && curb == 0) { flush(0); q = q1; curb = 1; }
      consume<10>(A, q, m, l, acc);
      uA = next_unit(read_ticket<9>(tA)); refill(A, tA, voff, kp + uoff(uA), vp + uoff(uA), cnt, uA);
      STAMPU(uA);
      if (uA >= UEND) ++fails;
    }
    if (uB < UEND) {
      if (fails) wait_n<0>();
      if (uB >= UPB && curb == 0) { flush(0); q = q1; curb = 1; }
      consume<10>(B, q, m, l, acc);
      uB = next_unit(read_ticket<9>(tB)); refill(B, tB, voff, kp + uoff(uB), vp + uoff(uB), cnt, uB);
      STAMPU(uB);
      if (uB >= UEND) ++fails;
    }
  }
  STAMPW(16 + wave, wave);
  if (curb == 0) flush(0);
  flush(1);
  __syncthreads();

  if (wave < 2) {
    const int bb = 1 - wave;
    const f32x4 m0 = *reinterpret_cast<const f32x4*>(&lds[L_MM + bb * NW]), m1 = *reinterpret_cast<const f32x4*>(&lds[L_MM + bb * NW + 4]);
    const f32x4 l0 = *reinterpret_cast<const f32x4*>(&lds[L_LL + bb * NW]), l1 = *reinterpret_cast<const f32x4*>(&lds[L_LL + bb * NW + 4]);
    const float M = fmaxf(fmaxf(fmaxf(m0[0], m0[1]), fmaxf(m0[2], m0[3])), fmaxf(fmaxf(m1[0], m1[1]), fmaxf(m1[2], m1[3])));
    float L = 0.f, a0 = 0.f, a1 = 0.f;
#pragma unroll
    for (int w = 0; w < NW; ++w) {
      const float wi = __builtin_amdgcn_exp2f((w < 4 ? m0[w & 3] : m1[w & 3]) - M);
      L = fmaf(wi, w < 4 ? l0[w & 3] : l1[w & 3], L);
      const f32x2 a2 = *reinterpret_cast<const f32x2*>(&lds[L_REC + (bb * NW + w) * DH + 2 * lane]);
      a0 = fmaf(wi, a2[0], a0); a1 = fmaf(wi, a2[1], a1);
    }
    st_dev((u32)(((((lane >> 3) * 2 + bb) * 8 + i) * 16 + 2 * (lane & 7)) * 8), ws + WS_P + (size_t)h * (8 * 2 * 8 * 128), a0, a1, tag);
    if (lane == 0) st_dev((u32)(((h * 2 + bb) * 8 + i) * 16), ws + WS_ML, M, L, tag);
  }
  STAMPW(4, 0);
  if (wave == 0) {
    const int bb = (lane >> 3) & 1, j = lane & 7;
    const u32 po = (u32)(((bb * 8 + j) * 16 + 4 * r) * 8), mo = (u32)(((h * 2 + bb) * 8 + j) * 16);
    const char* pb = ws + WS_P + (size_t)(h * 8 + i) * (2 * 8 * 128);
    u32x4 g0, g1, gm;
    for (u32 spins = 0; spins < SPIN_CAP; ++spins) {
      ld_dev(g0, po, pb); ld_dev(g1, po + 16, pb); ld_dev(gm, mo, ws + WS_ML);
      wait_1<0>(g0); wait_1<0>(g1); wait_1<0>(gm);
      const bool ok = g0[1] == tag && g0[3] == tag && g1[1] == tag && g1[3] == tag && gm[1] == tag && gm[3] == tag;
      if (__all(ok)) break;
      __builtin_amdgcn_s_sleep(1);
    }
    const float mj = __uint_as_float(gm[0]), lj = __uint_as_float(gm[2]);
    float Mh = fmaxf(mj, dpp<XOR1>(mj)); Mh = fmaxf(Mh, dpp<XOR2>(Mh)); Mh = fmaxf(Mh, dpp<XOR7>(Mh));
    const float wj = __builtin_amdgcn_exp2f(mj - Mh);
    float v[5] = {wj * lj, wj * __uint_as_float(g0[0]), wj * __uint_as_float(g0[2]), wj * __uint_as_float(g1[0]), wj * __uint_as_float(g1[2])};
#pragma unroll
    for (int e = 0; e < 5; ++e) { v[e] += dpp<XOR1>(v[e]); v[e] += dpp<XOR2>(v[e]); v[e] += dpp<XOR7>(v[e]); }
    if (j == 0) *reinterpret_cast<f32x4*>(&lds[L_O + bb * 16 + 4 * r]) = f32x4{bf16r(v[1] / v[0]), bf16r(v[2] / v[0]), bf16r(v[3] / v[0]), bf16r(v[4] / v[0])};
  } else if (is_red && wave >= 2) {
    const int e = tid - 128;
    if (e < NSLICE * 8) {
      u32x4 g;
      for (u32 spins = 0; spins < SPIN_CAP; ++spins) {
        ld_dev(g, (u32)(e * 16), ws + WS_F + (size_t)red * (NSLICE * 128));
        wait_1<0>(g);
        if (__all(g[1] == tag && g[3] == tag)) break;
        __builtin_amdgcn_s_sleep(4);
      }
      *reinterpret_cast<f32x2*>(&lds[L_BIG + 2 * e]) = f32x2{__uint_as_float(g[0]), __uint_as_float(g[2])};
    }
  }
  __syncthreads();
  STAMPW(5, 0);
  {
    const f32x4 o0 = *reinterpret_cast<const f32x4*>(&lds[L_O + 4 * r]), o1 = *reinterpret_cast<const f32x4*>(&lds[L_O + 16 + 4 * r]);
    float t0[4], t1[4];
#pragma unroll
    for (int e = 0; e < 4; ++e) { t0[e] = 0.f; t1[e] = 0.f; }
#pragma unroll
    for (int k = 0; k < 4; ++k) {
      const float w4[4] = {lo16(wor[k][0]), hi16(wor[k][0]), lo16(wor[k][1]), hi16(wor[k][1])};
#pragma unroll
      for (int e = 0; e < 4; ++e) { t0[e] = fmaf(o0[k], w4[e], t0[e]); t1[e] = fmaf(o1[k], w4[e], t1[e]); }
    }
#pragma unroll
    for (int e = 0; e < 4; ++e) { t0[e] = xrow_sum(t0[e]); t1[e] = xrow_sum(t1[e]); }
    const float sa = r == 0 ? t0[0] : r == 1 ? t0[2] : r == 2 ? t1[0] : t1[2];
    const float sb = r == 0 ? t0[1] : r == 1 ? t0[3] : r == 2 ? t1[1] : t1[3];
    st_dev((u32)(((8 * wave + (c >> 1)) * NWG + id) * 128 + (r >> 1) * 64 + ((c & 1) * 4 + (r & 1) * 2) * 8), ws + WS_T, sa, sb, tag);
  }
  if (!is_red) return;

  float gate = 0.f;
  if (tid < 16) {
    float d = 0.f;
#pragma unroll
    for (int s = 0; s < NSLICE; ++s) d += lds[L_BIG + s * 16 + tid];
    d = bf16r(d);
    gate = bf16r(d / (1.f + __expf(-d)));
  }
  {
    const char* tb = ws + WS_T + (size_t)red * (NWG * 128);
    u32x4 g[4];
    for (u32 spins = 0; spins < SPIN_CAP; ++spins) {
#pragma unroll
      for (int k = 0; k < 4; ++k) ld_dev(g[k], (u32)(tid * 16), tb + k * 8192);
      bool ok = true;
#pragma unroll
      for (int k = 0; k < 4; ++k) { wait_1<0>(g[k]); ok = ok && g[k][1] == tag && g[k][3] == tag; }
      if (__all(ok)) break;
      __builtin_amdgcn_s_sleep(1);
    }
    float s0 = 0.f, s1 = 0.f;
#pragma unroll
    for (int k = 0; k < 4; ++k) { s0 += __uint_as_float(g[k][0]); s1 += __uint_as_float(g[k][2]); }
    s0 += dpp<XOR8>(s0); s1 += dpp<XOR8>(s1);
    s0 = xrow_sum(s0); s1 = xrow_sum(s1);
    if (lane < 8) *reinterpret_cast<f32x2*>(&lds[L_FR + wave * 16 + 2 * lane]) = f32x2{s0, s1};
  }
  __syncthreads();
  if (tid < 16) {
    float s = 0.f;
#pragma unroll
    for (int w = 0; w < NW; ++w) s += lds[L_FR + w * 16 + tid];
    const int o = (tid >> 3) * DM + red * 8 + (tid & 7);
    const float xv = __uint_as_float((u32)x[o] << 16);
    out[o] = __builtin_bit_cast(unsigned short, (__bf16)(xv + bf16r(bf16r(s) * gate)));
    if (red == 0 && tid == 0) *(u32*)(ws + WS_EPOCH) = tag;
    STAMPW(6, 0);
  }
}

extern "C" void kernel_launch(void* const* d_in, const int* in_sizes, int n_in, void* d_out, int out_size, void* d_ws,
                              size_t ws_size, hipStream_t stream) {
  (void)in_sizes; (void)n_in; (void)out_size;
  if (ws_size < WS_END || ws_size < WS_CNT + (size_t)NH * CNT_STRIDE) return;
  fused_decode_kernel<<<NWG, NTHR, 0, stream>>>((const unsigned short*)d_in[0], (const char*)d_in[1], (const char*)d_in[2],
                                                (const char*)d_in[3], (const char*)d_in[4], (const char*)d_in[6],
                                                (const char*)d_in[7], (const char*)d_in[8], (unsigned short*)d_out, (char*)d_ws);
}
```

```cpp
#include <hip/hip_runtime.h>
#include <stdint.h>

typedef unsigned u32;
typedef unsigned long long u64;
typedef u32 u32x4 __attribute__((ext_vector_type(4)));
typedef u32 u32x2 __attribute__((ext_vector_type(2)));
typedef float f32x4 __attribute__((ext_vector_type(4)));
typedef float f32x2 __attribute__((ext_vector_type(2)));
typedef __bf16 bf16x2 __attribute__((ext_vector_type(2)));

constexpr int DM = 512, NH = 32, SK = 16384, DH = 128, FF = 1376;
constexpr int NW = 8, NTHR = NW * 64, NWG = 256;
constexpr int UKEYS = 16;
constexpr int UPB = SK / UKEYS;
constexpr int UEND = 2 * UPB;
constexpr int NSTAT = 4;
#define NL1 0
constexpr int NLOC = UPB / 8 + NL1;
constexpr int GLOB = UPB - 8 * NL1;
constexpr int NSLICE = 43, FS = 32;
constexpr int NRED = 64;
constexpr u32 SPIN_CAP = 1u << 20;
#define LD_WQ ld_nt
#define HOLD 100
#define POLL_AT 8

constexpr size_t WS_EPOCH = 0;
#define CNT_OFF 8388608
#define CNT_STRIDE 256
constexpr size_t WS_CNT = CNT_OFF;
constexpr size_t WS_Q = 16384;
constexpr size_t WS_ML = WS_Q + 2 * NH * 64 * 8;
constexpr size_t WS_P = WS_ML + NH * 2 * 8 * 16;
constexpr size_t WS_T = WS_P + (size_t)NH * 8 * 2 * 8 * 128;
constexpr size_t WS_F = WS_T + (size_t)NRED * NWG * 128;
constexpr size_t WS_END = WS_F + (size_t)NRED * NSLICE * 128;

constexpr int L_XN = 0;
constexpr int L_REC = L_XN + 2 * DM;
constexpr int L_MM = L_REC + 2 * NW * DH;
constexpr int L_LL = L_MM + 16;
constexpr int L_O = L_LL + 16;
constexpr int L_HS = L_O + 32;
constexpr int L_CNT = L_HS + 64;
constexpr int L_FR = L_CNT + 4;
constexpr int L_BIG = L_FR + 512;
constexpr int L_TK = L_BIG + 4 * 2 * DM;
constexpr int L_END = L_TK + 4;

__device__ __forceinline__ float bf16r(float x) { return (float)(__bf16)x; }
__device__ __forceinline__ float lo16(u32 w) { return __uint_as_float(w << 16); }
__device__ __forceinline__ float hi16(u32 w) { return __uint_as_float(w & 0xffff0000u); }
__device__ __forceinline__ float dot2(u32 a, u32 b, float c) {
  return __builtin_amdgcn_fdot2_f32_bf16(__builtin_bit_cast(bf16x2, a), __builtin_bit_cast(bf16x2, b), c, false);
}
template <int CTRL> __device__ __forceinline__ float dpp(float x) {
  return __builtin_bit_cast(float, __builtin_amdgcn_mov_dpp(__builtin_bit_cast(int, x), CTRL, 0xf, 0xf, true));
}
constexpr int XOR1 = 0xB1, XOR2 = 0x4E, XOR7 = 0x141, XOR8 = 0x128, ROR4 = 0x124;
__device__ __forceinline__ float row_sum16(float x) {
  x += dpp<XOR1>(x); x += dpp<XOR2>(x); x += dpp<XOR7>(x); x += dpp<XOR8>(x); return x;
}
__device__ __forceinline__ float row_sum_q4(float x) {
  x += dpp<ROR4>(x); x += dpp<XOR8>(x); return x;
}
__device__ __forceinline__ float xrow_max(float x) {
  auto s = __builtin_amdgcn_permlane16_swap(__float_as_uint(x), __float_as_uint(x), false, false);
  x = fmaxf(__uint_as_float(s[0]), __uint_as_float(s[1]));
  auto t = __builtin_amdgcn_permlane32_swap(__float_as_uint(x), __float_as_uint(x), false, false);
  return fmaxf(__uint_as_float(t[0]), __uint_as_float(t[1]));
}
__device__ __forceinline__ float xrow_sum(float x) {
  auto s = __builtin_amdgcn_permlane16_swap(__float_as_uint(x), __float_as_uint(x), false, false);
  x = __uint_as_float(s[0]) + __uint_as_float(s[1]);
  auto t = __builtin_amdgcn_permlane32_swap(__float_as_uint(x), __float_as_uint(x), false, false);
  return __uint_as_float(t[0]) + __uint_as_float(t[1]);
}
__device__ __forceinline__ float wave_sum(float x) { return xrow_sum(row_sum16(x)); }

#define STAMPW(i, w) do {} while (0)
#define STAMPU(u) do {} while (0)

struct Stage { u32x4 k[4]; u32x4 v[4]; };

#define STAGE_REGS(s) "+v"(s.k[0]), "+v"(s.k[1]), "+v"(s.k[2]), "+v"(s.k[3]), "+v"(s.v[0]), "+v"(s.v[1]), "+v"(s.v[2]), "+v"(s.v[3])
__device__ __forceinline__ void ld_plain(u32x4& d, u32 voff, const char* base) {
  asm volatile("s_nop 4\n\tglobal_load_dwordx4 %0, %1, %2" : "=&v"(d) : "v"(voff), "s"(base) : "memory");
}
__device__ __forceinline__ void ld_nt2(u32x2& d, u32 voff, const char* base) {
  asm volatile("s_nop 4\n\tglobal_load_dwordx2 %0, %1, %2 nt" : "=&v"(d) : "v"(voff), "s"(base) : "memory");
}
__device__ __forceinline__ void ld_nt(u32x4& d, u32 voff, const char* base) {
  asm volatile("s_nop 4\n\tglobal_load_dwordx4 %0, %1, %2 nt" : "=&v"(d) : "v"(voff), "s"(base) : "memory");
}
__device__ __forceinline__ void ld_dev(u32x4& d, u32 voff, const char* base) {
  asm volatile("s_nop 4\n\tglobal_load_dwordx4 %0, %1, %2 sc1" : "=&v"(d) : "v"(voff), "s"(base) : "memory");
}
__device__ __forceinline__ void st_dev(u32 voff, char* base, float a, float b, u32 tag) {
  const u32x4 d = {__float_as_uint(a), tag, __float_as_uint(b), tag};
  asm volatile("s_nop 4\n\tglobal_store_dwordx4 %0, %1, %2 sc1" :: "v"(voff), "v"(d), "s"(base) : "memory");
}
__device__ __forceinline__ void ticket(u32& t, const char* cnt) {
  asm volatile("s_nop 4\n\ts_mov_b64 exec, 1\n\tglobal_atomic_add %0, %1, %2, %3 sc0\n\ts_mov_b64 exec, -1"
               : "=&v"(t) : "v"(0u), "v"(1u), "s"(cnt) : "memory");
}
template <int N> __device__ __forceinline__ u32 read_ticket(u32 t) {
  u32 v;
  asm volatile("s_waitcnt vmcnt(%2)\n\tv_readfirstlane_b32 %0, %1\n\ts_nop 4" : "=s"(v) : "v"(t), "i"(N) : "memory");
  return v;
}
__device__ __forceinline__ void issue(Stage& s, u32 voff, const char* kb, const char* vb) {
  asm volatile("s_nop 4\n\t"
               "global_load_dwordx4 %0, %8, %9 nt\n\t"
               "global_load_dwordx4 %1, %8, %9 offset:1024 nt\n\t"
               "global_load_dwordx4 %2, %8, %9 offset:2048 nt\n\t"
               "global_load_dwordx4 %3, %8, %9 offset:3072 nt\n\t"
               "global_load_dwordx4 %4, %8, %10 nt\n\t"
               "global_load_dwordx4 %5, %8, %10 offset:1024 nt\n\t"
               "global_load_dwordx4 %6, %8, %10 offset:2048 nt\n\t"
               "global_load_dwordx4 %7, %8, %10 offset:3072 nt"
               : "=&v"(s.k[0]), "=&v"(s.k[1]), "=&v"(s.k[2]), "=&v"(s.k[3]), "=&v"(s.v[0]), "=&v"(s.v[1]), "=&v"(s.v[2]), "=&v"(s.v[3])
               : "v"(voff), "s"(kb), "s"(vb) : "memory");
}
__device__ __forceinline__ void refill(Stage& s, u32& t, u32 voff, const char* kb, const char* vb, const char* cnt, int u) {
  asm volatile("s_nop 4\n\t"
               "s_cmpk_lt_i32 %15, 0x800\n\t"
               "s_cbranch_scc0 1f\n\t"
               "global_load_dwordx4 %0, %9, %10 nt\n\t"
               "global_load_dwordx4 %1, %9, %10 offset:1024 nt\n\t"
               "global_load_dwordx4 %2, %9, %10 offset:2048 nt\n\t"
               "global_load_dwordx4 %3, %9, %10 offset:3072 nt\n\t"
               "global_load_dwordx4 %4, %9, %11 nt\n\t"
               "global_load_dwordx4 %5, %9, %11 offset:1024 nt\n\t"
               "global_load_dwordx4 %6, %9, %11 offset:2048 nt\n\t"
               "global_load_dwordx4 %7, %9, %11 offset:3072 nt\n\t"
               "s_mov_b64 exec, 1\n\t"
               "global_atomic_add %8, %13, %14, %12 sc0\n\t"
               "s_mov_b64 exec, -1\n\t"
               "s_branch 2f\n"
               "1:\n\t"
               "s_waitcnt vmcnt(0)\n"
               "2:"
               : STAGE_REGS(s), "=&v"(t) : "v"(voff), "s"(kb), "s"(vb), "s"(cnt), "v"(0u), "v"(1u), "s"(u) : "memory", "scc");
}
__device__ __forceinline__ void refill_own(Stage& s, u32& t, u32 voff, const char* kb, const char* vb, const char* cnt, int g) {
  asm volatile("s_nop 4\n\t"
               "global_load_dwordx4 %0, %9, %10 nt\n\t"
               "global_load_dwordx4 %1, %9, %10 offset:1024 nt\n\t"
               "global_load_dwordx4 %2, %9, %10 offset:2048 nt\n\t"
               "global_load_dwordx4 %3, %9, %10 offset:3072 nt\n\t"
               "global_load_dwordx4 %4, %9, %11 nt\n\t"
               "global_load_dwordx4 %5, %9, %11 offset:1024 nt\n\t"
               "global_load_dwordx4 %6, %9, %11 offset:2048 nt\n\t"
               "global_load_dwordx4 %7, %9, %11 offset:3072 nt\n\t"
               "s_cmpk_lt_i32 %15, %16\n\t"
               "s_cbranch_scc1 1f\n\t"
               "s_mov_b64 exec, 1\n\t"
               "global_atomic_add %8, %13, %14, %12 sc0\n\t"
               "s_mov_b64 exec, -1\n"
               "1:"
               : STAGE_REGS(s), "+v"(t) : "v"(voff), "s"(kb), "s"(vb), "s"(cnt), "v"(0u), "v"(1u), "s"(g), "i"(NLOC - 2 * NW) : "memory", "scc");
}
static_assert(UEND == 0x800, "refill compares against 0x800");
template <int N> __device__ __forceinline__ void wait_k(Stage& s) {
  asm volatile("s_waitcnt vmcnt(%4)" : "+v"(s.k[0]), "+v"(s.k[1]), "+v"(s.k[2]), "+v"(s.k[3]) : "i"(N) : "memory");
}
template <int N> __device__ __forceinline__ void wait_v(Stage& s) {
  asm volatile("s_waitcnt vmcnt(%4)" : "+v"(s.v[0]), "+v"(s.v[1]), "+v"(s.v[2]), "+v"(s.v[3]) : "i"(N) : "memory");
}
template <int N, typename V> __device__ __forceinline__ void wait_1(V& d) { asm volatile("s_waitcnt vmcnt(%1)" : "+v"(d) : "i"(N) : "memory"); }
template <int N> __device__ __forceinline__ void wait_n() { asm volatile("s_waitcnt vmcnt(%0)" :: "i"(N) : "memory"); }

template <int LATER>
__device__ __forceinline__ void consume(Stage& s, const u32x4 q, float& m, float& l, float (&acc)[8]) {
  constexpr float SCALE = 0.08838834764831845f, LOG2E = 1.4426950408889634f;
  float y[4];
  wait_k<LATER + 4>(s);
#pragma unroll
  for (int t = 0; t < 4; ++t) {
    float d = dot2(s.k[t][0], q[0], 0.f);
    d = dot2(s.k[t][1], q[1], d); d = dot2(s.k[t][2], q[2], d); d = dot2(s.k[t][3], q[3], d);
    d = row_sum16(d);
    y[t] = bf16r(bf16r(d) * SCALE) * LOG2E;
  }
  float mn = m;
#pragma unroll
  for (int t = 0; t < 4; ++t) mn = fmaxf(mn, y[t]);
  const float alpha = __builtin_amdgcn_exp2f(m - mn);
  float ps = 0.f;
#pragma unroll
  for (int e = 0; e < 8; ++e) acc[e] *= alpha;
  wait_v<LATER>(s);
#pragma unroll
  for (int t = 0; t < 4; ++t) {
    const float p = __builtin_amdgcn_exp2f(y[t] - mn);
    ps += p;
#pragma unroll
    for (int e = 0; e < 4; ++e) {
      acc[2 * e] = fmaf(p, lo16(s.v[t][e]), acc[2 * e]);
      acc[2 * e + 1] = fmaf(p, hi16(s.v[t][e]), acc[2 * e + 1]);
    }
  }
  l = fmaf(l, alpha, ps);
  m = mn;
  asm volatile("" : "+v"(acc[0]), "+v"(acc[1]), "+v"(acc[2]), "+v"(acc[3]), "+v"(acc[4]), "+v"(acc[5]), "+v"(acc[6]), "+v"(acc[7]), "+v"(l), "+v"(m));
  __builtin_amdgcn_sched_barrier(0);
}

constexpr float M_NONE = -1.0e30f;

__device__ __forceinline__ u32 lds_inc(float* lds, int idx) {
  return __hip_atomic_fetch_add(&reinterpret_cast<u32*>(lds)[idx], 1u, __ATOMIC_RELAXED, __HIP_MEMORY_SCOPE_WORKGROUP);
}

__device__ __forceinline__ void layer_norm(const u32x4 (&xr)[2], float* lds, int lane) {
#pragma unroll
  for (int bb = 0; bb < 2; ++bb) {
    float xv[8];
#pragma unroll
    for (int e = 0; e < 4; ++e) { xv[2 * e] = lo16(xr[bb][e]); xv[2 * e + 1] = hi16(xr[bb][e]); }
    float s = 0.f;
#pragma unroll
    for (int e = 0; e < 8; ++e) s += xv[e];
    const float mean = wave_sum(s) * (1.f / DM);
    float s2 = 0.f;
#pragma unroll
    for (int e = 0; e < 8; ++e) { const float d = xv[e] - mean; s2 = fmaf(d, d, s2); }
    const float var = bf16r(wave_sum(s2) * (1.f / DM));
    const float mb = bf16r(mean);
    const float den = bf16r(sqrtf(bf16r(var + 1e-5f)));
    f32x4 o0, o1;
#pragma unroll
    for (int e = 0; e < 4; ++e) { o0[e] = bf16r(bf16r(xv[e] - mb) / den); o1[e] = bf16r(bf16r(xv[4 + e] - mb) / den); }
    *reinterpret_cast<f32x4*>(&lds[L_XN + bb * DM + lane * 8]) = o0;
    *reinterpret_cast<f32x4*>(&lds[L_XN + bb * DM + lane * 8 + 4]) = o1;
  }
  asm volatile("s_waitcnt lgkmcnt(0)" ::: "memory");
}

__global__ void __launch_bounds__(NTHR)
fused_decode_kernel(const unsigned short* __restrict__ x, const char* __restrict__ kc, const char* __restrict__ vc,
                    const char* __restrict__ wq, const char* __restrict__ wo, const char* __restrict__ wg,
                    const char* __restrict__ wu, const char* __restrict__ wd, unsigned short* __restrict__ out,
                    char* __restrict__ ws) {
  __shared__ __attribute__((aligned(16))) float lds[L_END];
  const int tid = threadIdx.x, lane = tid & 63, wave = __builtin_amdgcn_readfirstlane(tid >> 6);
  const int r = lane >> 4, c = lane & 15;
  const int id = blockIdx.x, i = id & 7, h = id >> 3;
  int slice = -1;
  if (i == ((h >> 1) & 7)) slice = h;
  else if (h % 3 == 0 && i == (((h / 6) * 3 + 5) & 7)) slice = 32 + h / 3;
  const int dd = ((h >> 1) - i) & 7;
  const bool is_q = (dd & 3) == 2;
  const bool ffn_wave = slice >= 0 && wave >= 1 && wave <= 4;
  const bool is_red = (i & 3) == (h & 3);
  const int red = 2 * h + (i >> 2);
  const u32 voff = lane * 16;
  const u32 tag = *(const u32*)(ws + WS_EPOCH) + 1u;
  const u32 ctag = (tag ^ 0x80000u) & 0xfffffu;
  const char* cnt = ws + WS_CNT + (size_t)h * CNT_STRIDE;
  STAMPW(0, 0);
  if (tid < 4) reinterpret_cast<u32*>(lds)[L_CNT + tid] = 0u;
  if (tid == 4) reinterpret_cast<u32*>(lds)[L_TK] = (u32)(NW * (NSTAT + 2));
  __syncthreads();

  if (i == 0 && tid == 0)
    __hip_atomic_store((__attribute__((address_space(1))) u32*)(ws + WS_CNT + (size_t)h * CNT_STRIDE), ctag << 12,
                       __ATOMIC_RELAXED, __HIP_MEMORY_SCOPE_AGENT);
  u32x2 wor[4];
  auto load_wo = [&]() {
#pragma unroll
    for (int k = 0; k < 4; ++k) ld_nt2(wor[k], (u32)((4 * r + k) * (DM * 2) + c * 8), wo + (size_t)(h * DH + 16 * i) * (DM * 2) + wave * 128);
  };
  load_wo();
  if (!is_q && !ffn_wave) {
    const u64 t_start = __builtin_amdgcn_s_memrealtime();
    const size_t hb = (size_t)h * SK * DH * 2 + (size_t)(i * (UPB / 8) + wave) * (UKEYS * DH * 2);
    u32 t0, t1, t2;
    asm volatile("global_load_dword %0, %3, %4 nt\n\tglobal_load_dword %1, %3, %5 nt\n\tglobal_load_dword %2, %3, %6 sc1"
                 : "=&v"(t0), "=&v"(t1), "=&v"(t2) : "v"(0u), "s"(kc + hb), "s"(vc + hb), "s"(ws + WS_Q + h * 512) : "memory");
    for (int spins = 0; spins < 4096 && __builtin_amdgcn_s_memrealtime() - t_start < HOLD; ++spins) __builtin_amdgcn_s_sleep(2);
    asm volatile("s_waitcnt vmcnt(0)" : "+v"(t0), "+v"(t1), "+v"(t2) :: "memory");
  }
  if (is_q) {
    const int qi = 2 * h + (dd >> 2);
    u32x4 xr[2], wr[8];
    ld_plain(xr[0], voff, (const char*)x);
    ld_plain(xr[1], voff, (const char*)x + 1024);
    {
      const u32 vo = (lane >> 3) * (NH * DH * 2) + (lane & 7) * 16;
#pragma unroll
      for (int k = 0; k < 8; ++k) LD_WQ(wr[k], vo, wq + qi * 128 + (size_t)(64 * wave + 8 * k) * (NH * DH * 2));
    }
    wait_1<8>(xr[0]); wait_1<8>(xr[1]);
    layer_norm(xr, lds, lane);
    float qa[2][8];
#pragma unroll
    for (int e = 0; e < 8; ++e) { qa[0][e] = 0.f; qa[1][e] = 0.f; }
    for (int z = 0; z < 8; ++z) wait_1<0>(wr[z]);
#pragma unroll
    for (int k = 0; k < 8; ++k) {
      const float x0 = lds[L_XN + 64 * wave + (lane >> 3) + 8 * k], x1 = lds[L_XN + DM + 64 * wave + (lane >> 3) + 8 * k];
#pragma unroll
      for (int e = 0; e < 4; ++e) {
        const float a = lo16(wr[k][e]), bq = hi16(wr[k][e]);
        qa[0][2 * e] = fmaf(x0, a, qa[0][2 * e]); qa[0][2 * e + 1] = fmaf(x0, bq, qa[0][2 * e + 1]);
        qa[1][2 * e] = fmaf(x1, a, qa[1][2 * e]); qa[1][2 * e + 1] = fmaf(x1, bq, qa[1][2 * e + 1]);
      }
    }
#pragma unroll
    for (int bb = 0; bb < 2; ++bb) {
#pragma unroll
      for (int e = 0; e < 8; ++e) { const float t = qa[bb][e] + dpp<XOR8>(qa[bb][e]); qa[bb][e] = xrow_sum(t); }
      if (lane < 8) {
        float* rec = &lds[L_BIG + (wave * 2 + bb) * 64 + lane * 8];
        *reinterpret_cast<f32x4*>(rec) = f32x4{qa[bb][0], qa[bb][1], qa[bb][2], qa[bb][3]};
        *reinterpret_cast<f32x4*>(rec + 4) = f32x4{qa[bb][4], qa[bb][5], qa[bb][6], qa[bb][7]};
      }
    }
    asm volatile("s_waitcnt lgkmcnt(0)" ::: "memory");
    u32 arr = 0;
    if (lane == 0) arr = lds_inc(lds, L_CNT + 3);
    if (__builtin_amdgcn_readfirstlane(arr) == NW - 1) {
      asm volatile("" ::: "memory");
      if (lane < 32) {
        f32x4 t = {0.f, 0.f, 0.f, 0.f};
#pragma unroll
        for (int w = 0; w < NW; ++w) t += *reinterpret_cast<const f32x4*>(&lds[L_BIG + w * 128 + lane * 4]);
        const bf16x2 p0 = {(__bf16)t[0], (__bf16)t[1]}, p1 = {(__bf16)t[2], (__bf16)t[3]};
        st_dev((u32)(((lane >> 4) * NH * 64 + qi * 32 + (lane & 15) * 2) * 8), ws + WS_Q,
               __builtin_bit_cast(float, p0), __builtin_bit_cast(float, p1), tag);
      }
      STAMPW(7, wave);
    }
  } else if (ffn_wave) {
    const int fw = wave - 1, ft = tid - 64;
    u32x4 xr[2], gw[8], uw[8], dw[8];
    ld_plain(xr[0], voff, (const char*)x);
    ld_plain(xr[1], voff, (const char*)x + 1024);
    {
      const u32 vo = (u32)((ft >> 2) * (FF * 2) + (ft & 3) * 16);
#pragma unroll
      for (int k = 0; k < 8; ++k) ld_nt(gw[k], vo, wg + slice * (FS * 2) + (size_t)k * 64 * (FF * 2));
#pragma unroll
      for (int k = 0; k < 8; ++k) ld_nt(uw[k], vo, wu + slice * (FS * 2) + (size_t)k * 64 * (FF * 2));
#pragma unroll
      for (int k = 0; k < 8; ++k) ld_nt(dw[k], voff, wd + (size_t)(slice * FS + 8 * fw + k) * (DM * 2));
    }
    wait_1<24>(xr[0]); wait_1<24>(xr[1]);
    layer_norm(xr, lds, lane);
    for (int z = 0; z < 8; ++z) { wait_1<8>(gw[z]); wait_1<8>(uw[z]); }
#pragma unroll
    for (int bb = 0; bb < 2; ++bb) {
      float ga[8], ua[8];
#pragma unroll
      for (int e = 0; e < 8; ++e) { ga[e] = 0.f; ua[e] = 0.f; }
#pragma unroll
      for (int k = 0; k < 8; ++k) {
        const float xv = lds[L_XN + bb * DM + (ft >> 2) + 64 * k];
#pragma unroll
        for (int e = 0; e < 4; ++e) {
          ga[2 * e] = fmaf(xv, lo16(gw[k][e]), ga[2 * e]); ga[2 * e + 1] = fmaf(xv, hi16(gw[k][e]), ga[2 * e + 1]);
          ua[2 * e] = fmaf(xv, lo16(uw[k][e]), ua[2 * e]); ua[2 * e + 1] = fmaf(xv, hi16(uw[k][e]), ua[2 * e + 1]);
        }
      }
#pragma unroll
      for (int e = 0; e < 8; ++e) { ga[e] = xrow_sum(row_sum_q4(ga[e])); ua[e] = xrow_sum(row_sum_q4(ua[e])); }
      if (lane < 4) {
        float* rec = &lds[L_FR + fw * 128 + bb * 64 + lane * 8];
        *reinterpret_cast<f32x4*>(rec) = f32x4{ga[0], ga[1], ga[2], ga[3]};
        *reinterpret_cast<f32x4*>(rec + 4) = f32x4{ga[4], ga[5], ga[6], ga[7]};
        *reinterpret_cast<f32x4*>(rec + 32) = f32x4{ua[0], ua[1], ua[2], ua[3]};
        *reinterpret_cast<f32x4*>(rec + 36) = f32x4{ua[4], ua[5], ua[6], ua[7]};
      }
    }
    asm volatile("s_waitcnt lgkmcnt(0)" ::: "memory");
    u32 arr = 0;
    if (lane == 0) arr = lds_inc(lds, L_CNT + 0);
    if (__builtin_amdgcn_readfirstlane(arr) == 3) {
      asm volatile("" ::: "memory");
      const int bb = lane >> 5, col = lane & 31;
      float g = 0.f, u = 0.f;
#pragma unroll
      for (int w = 0; w < 4; ++w) { g += lds[L_FR + w * 128 + bb * 64 + col]; u += lds[L_FR + w * 128 + bb * 64 + 32 + col]; }
      g = bf16r(g); u = bf16r(u);
      const float inner = 0.7978845608028654f * (g + 0.044715f * g * g * g);
      const float ge = bf16r(0.5f * g * (1.f + tanhf(inner)));
      lds[L_HS + lane] = bf16r(ge * u);
      asm volatile("s_waitcnt lgkmcnt(0)" ::: "memory");
      if (lane == 0) __hip_atomic_store(&reinterpret_cast<u32*>(lds)[L_CNT + 1], 1u, __ATOMIC_RELAXED, __HIP_MEMORY_SCOPE_WORKGROUP);
    }
    for (u32 spins = 0; spins < SPIN_CAP; ++spins) {
      if (__hip_atomic_load(&reinterpret_cast<u32*>(lds)[L_CNT + 1], __ATOMIC_RELAXED, __HIP_MEMORY_SCOPE_WORKGROUP) != 0u) break;
      __builtin_amdgcn_s_sleep(1);
    }
    asm volatile("" ::: "memory");
    for (int z = 0; z < 8; ++z) wait_1<0>(dw[z]);
#pragma unroll
    for (int bb = 0; bb < 2; ++bb) {
      float da[8];
#pragma unroll
      for (int e = 0; e < 8; ++e) da[e] = 0.f;
#pragma unroll
      for (int k = 0; k < 8; ++k) {
        const float hv = lds[L_HS + bb * 32 + 8 * fw + k];
#pragma unroll
        for (int e = 0; e < 4; ++e) { da[2 * e] = fmaf(hv, lo16(dw[k][e]), da[2 * e]); da[2 * e + 1] = fmaf(hv, hi16(dw[k][e]), da[2 * e + 1]); }
      }
      float* rec = &lds[L_BIG + (fw * 2 + bb) * DM + lane * 8];
      *reinterpret_cast<f32x4*>(rec) = f32x4{da[0], da[1], da[2], da[3]};
      *reinterpret_cast<f32x4*>(rec + 4) = f32x4{da[4], da[5], da[6], da[7]};
    }
    asm volatile("s_waitcnt lgkmcnt(0)" ::: "memory");
    if (lane == 0) arr = lds_inc(lds, L_CNT + 2);
    if (__builtin_amdgcn_readfirstlane(arr) == 3) {
      asm volatile("" ::: "memory");
      const int pc = lane & 7;
#pragma unroll
      for (int k = 0; k < 8; ++k) {
        const int rd = 8 * k + (lane >> 3);
        const int idx = (pc >> 2) * DM + rd * 8 + 2 * (pc & 3);
        float s0 = 0.f, s1 = 0.f;
#pragma unroll
        for (int w = 0; w < 4; ++w) { const f32x2 t2 = *reinterpret_cast<const f32x2*>(&lds[L_BIG + w * 2 * DM + idx]); s0 += t2[0]; s1 += t2[1]; }
        st_dev((u32)((rd * NSLICE + slice) * 128 + pc * 16), ws + WS_F, s0, s1, tag);
      }
    }
    STAMPW(2, 1);
  }

  constexpr size_t UB = (size_t)UKEYS * DH * 2;
  const char* kp = kc + (size_t)h * SK * DH * 2;
  const char* vp = vc + (size_t)h * SK * DH * 2;
  auto uoff = [&](int u) -> size_t {
    u = min(u, UEND - 1);
    return (size_t)(u & (UPB - 1)) * UB + (size_t)(u >> 10) * ((size_t)NH * SK * DH * 2);
  };
  Stage A, B, C, D;
  const int us = i * (UPB / 8) + wave;
  issue(A, voff, kp + us * UB, vp + us * UB);
  issue(B, voff, kp + (NW + us) * UB, vp + (NW + us) * UB);
  issue(C, voff, kp + (2 * NW + us) * UB, vp + (2 * NW + us) * UB);
  STAMPW(24 + wave, wave);
  wait_n<POLL_AT>();
  u32 tA = 0, tB = 0;
  u32x4 q0, q1;
  {
    u32x4 g[4];
    const u32 qo = (u32)((h * 64 + 4 * c) * 8);
    ld_dev(g[0], qo, ws + WS_Q); ld_dev(g[1], qo + 16, ws + WS_Q);
    ld_dev(g[2], qo, ws + WS_Q + NH * 64 * 8); ld_dev(g[3], qo + 16, ws + WS_Q + NH * 64 * 8);
    issue(D, voff, kp + (3 * NW + us) * UB, vp + (3 * NW + us) * UB);
    wait_1<8>(g[0]); wait_1<8>(g[1]); wait_1<8>(g[2]); wait_1<8>(g[3]);
    for (u32 spins = 0; spins < SPIN_CAP; ++spins) {
      bool ok = true;
#pragma unroll
      for (int k = 0; k < 4; ++k) ok = ok && g[k][1] == tag && g[k][3] == tag;
      if (__all(ok)) break;
      __builtin_amdgcn_s_sleep(2);
      ld_dev(g[0], qo, ws + WS_Q); ld_dev(g[1], qo + 16, ws + WS_Q);
      ld_dev(g[2], qo, ws + WS_Q + NH * 64 * 8); ld_dev(g[3], qo + 16, ws + WS_Q + NH * 64 * 8);
      wait_1<0>(g[0]); wait_1<0>(g[1]); wait_1<0>(g[2]); wait_1<0>(g[3]);
    }
    q0 = u32x4{g[0][0], g[0][2], g[1][0], g[1][2]};
    q1 = u32x4{g[2][0], g[2][2], g[3][0], g[3][2]};
  }
  STAMPW(8 + wave, wave);
  float m = M_NONE, l = 0.f, acc[8];
#pragma unroll
  for (int e = 0; e < 8; ++e) acc[e] = 0.f;
  auto next_unit = [&](u32 val) -> int {
    for (u32 spins = 0; (val >> 12) != ctag; ++spins) {
      if (spins >= SPIN_CAP) return UEND;
      u32 t2;
      ticket(t2, cnt); val = read_ticket<0>(t2);
    }
    return (val & 0xfffu) < (u32)GLOB ? UPB + (int)(val & 0xfffu) : UEND;
  };
  auto flush = [&](int bb) {
    const float mw = xrow_max(m);
    const float sc = __builtin_amdgcn_exp2f(m - mw);
    const float lw = xrow_sum(l * sc);
    float a[8];
#pragma unroll
    for (int e = 0; e < 8; ++e) a[e] = xrow_sum(acc[e] * sc);
    if (r == 0) {
      float* rec = &lds[L_REC + (bb * NW + wave) * DH + 8 * c];
      *reinterpret_cast<f32x4*>(rec) = f32x4{a[0], a[1], a[2], a[3]};
      *reinterpret_cast<f32x4*>(rec + 4) = f32x4{a[4], a[5], a[6], a[7]};
      if (c == 0) { lds[L_MM + bb * NW + wave] = mw; lds[L_LL + bb * NW + wave] = lw; }
    }
    m = M_NONE; l = 0.f;
#pragma unroll
    for (int e = 0; e < 8; ++e) acc[e] = 0.f;
  };
  int uA, uB;
  for (int k = 0; k < 4; ++k) wait_1<8>(wor[k]);
  auto draw = [&]() -> u32 { u32 n = 0; if (lane == 0) n = lds_inc(lds, L_TK); return n; };
  auto local_unit = [&](int n) -> int { return n < UPB / 8 ? i * (UPB / 8) + n : UPB + GLOB + NL1 * i + (n - UPB / 8); };
  uA = us + 4 * NW; uB = us + 5 * NW;
  consume<8>(A, q0, m, l, acc); issue(A, voff, kp + uA * UB, vp + uA * UB);
  consume<8>(B, q0, m, l, acc); issue(B, voff, kp + uB * UB, vp + uB * UB);
  consume<16>(C, q0, m, l, acc);
  consume<16>(D, q0, m, l, acc);
  u32x4 q = q0;
  int curb = 0;
  static_assert(NLOC - 2 * NW >= NW * (NSTAT + 2), "window");
  bool gB = false, skipA = false;
  for (;;) {
    const u32 dA = draw();
    if (uA >= UPB && curb == 0) { flush(0); q = q1; curb = 1; }
    consume<8>(A, q, m, l, acc);
    const int nA = __builtin_amdgcn_readfirstlane(dA);
    uA = local_unit(nA);
    const bool gA = nA >= NLOC - 2 * NW;
    refill_own(A, tA, voff, kp + uoff(uA), vp + uoff(uA), cnt, nA);
    if (gB) { skipA = true; break; }
    const u32 dB = draw();
    if (uB >= UPB && curb == 0) { flush(0); q = q1; curb = 1; }
    consume<8>(B, q, m, l, acc);
    const int nB = __builtin_amdgcn_readfirstlane(dB);
    uB = local_unit(nB);
    gB = nB >= NLOC - 2 * NW;
    refill_own(B, tB, voff, kp + uoff(uB), vp + uoff(uB), cnt, nB);
    if (gA) break;
  }
  int fails = (uA >= UEND ? 1 : 0) + (uB >= UEND ? 1 : 0);
  while (fails < 2) {
    if (uA < UEND && !skipA) {
      if (fails) wait_n<0>();
      if (uA >= UPB && curb == 0) { flush(0); q = q1; curb = 1; }
      consume<10>(A, q, m, l, acc);
      uA = next_unit(read_ticket<9>(tA)); refill(A, tA, voff, kp + uoff(uA), vp + uoff(uA), cnt, uA);
      STAMPU(uA);
      if (uA >= UEND) ++fails;
    }
    skipA = false;
    if (uB < UEND) {
      if (fails) wait_n<0>();
      if (uB >= UPB && curb == 0) { flush(0); q = q1; curb = 1; }
      consume<10>(B, q, m, l, acc);
      uB = next_unit(read_ticket<9>(tB)); refill(B, tB, voff, kp + uoff(uB), vp + uoff(uB), cnt, uB);
      STAMPU(uB);
      if (uB >= UEND) ++fails;
    }
  }
  STAMPW(16 + wave, wave);
  if (curb == 0) flush(0);
  flush(1);
  __syncthreads();

  if (wave < 2) {
    const int bb = 1 - wave;
    const f32x4 m0 = *reinterpret_cast<const f32x4*>(&lds[L_MM + bb * NW]), m1 = *reinterpret_cast<const f32x4*>(&lds[L_MM + bb * NW + 4]);
    const f32x4 l0 = *reinterpret_cast<const f32x4*>(&lds[L_LL + bb * NW]), l1 = *reinterpret_cast<const f32x4*>(&lds[L_LL + bb * NW + 4]);
    const float M = fmaxf(fmaxf(fmaxf(m0[0], m0[1]), fmaxf(m0[2], m0[3])), fmaxf(fmaxf(m1[0], m1[1]), fmaxf(m1[2], m1[3])));
    float L = 0.f, a0 = 0.f, a1 = 0.f;
#pragma unroll
    for (int w = 0; w < NW; ++w) {
      const float wi = __builtin_amdgcn_exp2f((w < 4 ? m0[w & 3] : m1[w & 3]) - M);
      L = fmaf(wi, w < 4 ? l0[w & 3] : l1[w & 3], L);
      const f32x2 a2 = *reinterpret_cast<const f32x2*>(&lds[L_REC + (bb * NW + w) * DH + 2 * lane]);
      a0 = fmaf(wi, a2[0], a0); a1 = fmaf(wi, a2[1], a1);
    }
    st_dev((u32)(((((lane >> 3) * 2 + bb) * 8 + i) * 16 + 2 * (lane & 7)) * 8), ws + WS_P + (size_t)h * (8 * 2 * 8 * 128), a0, a1, tag);
    if (lane == 0) st_dev((u32)(((h * 2 + bb) * 8 + i) * 16), ws + WS_ML, M, L, tag);
  }
  STAMPW(4, 0);
  if (wave == 0) {
    const int bb = (lane >> 3) & 1, j = lane & 7;
    const u32 po = (u32)(((bb * 8 + j) * 16 + 4 * r) * 8), mo = (u32)(((h * 2 + bb) * 8 + j) * 16);
    const char* pb = ws + WS_P + (size_t)(h * 8 + i) * (2 * 8 * 128);
    u32x4 g0, g1, gm;
    for (u32 spins = 0; spins < SPIN_CAP; ++spins) {
      ld_dev(g0, po, pb); ld_dev(g1, po + 16, pb); ld_dev(gm, mo, ws + WS_ML);
      wait_1<0>(g0); wait_1<0>(g1); wait_1<0>(gm);
      const bool ok = g0[1] == tag && g0[3] == tag && g1[1] == tag && g1[3] == tag && gm[1] == tag && gm[3] == tag;
      if (__all(ok)) break;
      __builtin_amdgcn_s_sleep(1);
    }
    const float mj = __uint_as_float(gm[0]), lj = __uint_as_float(gm[2]);
    float Mh = fmaxf(mj, dpp<XOR1>(mj)); Mh = fmaxf(Mh, dpp<XOR2>(Mh)); Mh = fmaxf(Mh, dpp<XOR7>(Mh));
    const float wj = __builtin_amdgcn_exp2f(mj - Mh);
    float v[5] = {wj * lj, wj * __uint_as_float(g0[0]), wj * __uint_as_float(g0[2]), wj * __uint_as_float(g1[0]), wj * __uint_as_float(g1[2])};
#pragma unroll
    for (int e = 0; e < 5; ++e) { v[e] += dpp<XOR1>(v[e]); v[e] += dpp<XOR2>(v[e]); v[e] += dpp<XOR7>(v[e]); }
    if (j == 0) *reinterpret_cast<f32x4*>(&lds[L_O + bb * 16 + 4 * r]) = f32x4{bf16r(v[1] / v[0]), bf16r(v[2] / v[0]), bf16r(v[3] / v[0]), bf16r(v[4] / v[0])};
  } else if (is_red && wave >= 2) {
    const int e = tid - 128;
    if (e < NSLICE * 8) {
      u32x4 g;
      for (u32 spins = 0; spins < SPIN_CAP; ++spins) {
        ld_dev(g, (u32)(e * 16), ws + WS_F + (size_t)red * (NSLICE * 128));
        wait_1<0>(g);
        if (__all(g[1] == tag && g[3] == tag)) break;
        __builtin_amdgcn_s_sleep(4);
      }
      *reinterpret_cast<f32x2*>(&lds[L_BIG + 2 * e]) = f32x2{__uint_as_float(g[0]), __uint_as_float(g[2])};
    }
  }
  __syncthreads();
  STAMPW(5, 0);
  {
    const f32x4 o0 = *reinterpret_cast<const f32x4*>(&lds[L_O + 4 * r]), o1 = *reinterpret_cast<const f32x4*>(&lds[L_O + 16 + 4 * r]);
    float t0[4], t1[4];
#pragma unroll
    for (int e = 0; e < 4; ++e) { t0[e] = 0.f; t1[e] = 0.f; }
#pragma unroll
    for (int k = 0; k < 4; ++k) {
      const float w4[4] = {lo16(wor[k][0]), hi16(wor[k][0]), lo16(wor[k][1]), hi16(wor[k][1])};
#pragma unroll
      for (int e = 0; e < 4; ++e) { t0[e] = fmaf(o0[k], w4[e], t0[e]); t1[e] = fmaf(o1[k], w4[e], t1[e]); }
    }
#pragma unroll
    for (int e = 0; e < 4; ++e) { t0[e] = xrow_sum(t0[e]); t1[e] = xrow_sum(t1[e]); }
    const float sa = r == 0 ? t0[0] : r == 1 ? t0[2] : r == 2 ? t1[0] : t1[2];
    const float sb = r == 0 ? t0[1] : r == 1 ? t0[3] : r == 2 ? t1[1] : t1[3];
    st_dev((u32)(((8 * wave + (c >> 1)) * NWG + id) * 128 + (r >> 1) * 64 + ((c & 1) * 4 + (r & 1) * 2) * 8), ws + WS_T, sa, sb, tag);
  }
  if (!is_red) return;

  float gate = 0.f;
  if (tid < 16) {
    float d = 0.f;
#pragma unroll
    for (int s = 0; s < NSLICE; ++s) d += lds[L_BIG + s * 16 + tid];
    d = bf16r(d);
    gate = bf16r(d / (1.f + __expf(-d)));
  }
  {
    const char* tb = ws + WS_T + (size_t)red * (NWG * 128);
    u32x4 g[4];
    for (u32 spins = 0; spins < SPIN_CAP; ++spins) {
#pragma unroll
      for (int k = 0; k < 4; ++k) ld_dev(g[k], (u32)(tid * 16), tb + k * 8192);
      bool ok = true;
#pragma unroll
      for (int k = 0; k < 4; ++k) { wait_1<0>(g[k]); ok = ok && g[k][1] == tag && g[k][3] == tag; }
      if (__all(ok)) break;
      __builtin_amdgcn_s_sleep(1);
    }
    float s0 = 0.f, s1 = 0.f;
#pragma unroll
    for (int k = 0; k < 4; ++k) { s0 += __uint_as_float(g[k][0]); s1 += __uint_as_float(g[k][2]); }
    s0 += dpp<XOR8>(s0); s1 += dpp<XOR8>(s1);
    s0 = xrow_sum(s0); s1 = xrow_sum(s1);
    if (lane < 8) *reinterpret_cast<f32x2*>(&lds[L_FR + wave * 16 + 2 * lane]) = f32x2{s0, s1};
  }
  __syncthreads();
  if (tid < 16) {
    float s = 0.f;
#pragma unroll
    for (int w = 0; w < NW; ++w) s += lds[L_FR + w * 16 + tid];
    const int o = (tid >> 3) * DM + red * 8 + (tid & 7);
    const float xv = __uint_as_float((u32)x[o] << 16);
    out[o] = __builtin_bit_cast(unsigned short, (__bf16)(xv + bf16r(bf16r(s) * gate)));
    if (red == 0 && tid == 0) *(u32*)(ws + WS_EPOCH) = tag;
    STAMPW(6, 0);
  }
}

extern "C" void kernel_launch(void* const* d_in, const int* in_sizes, int n_in, void* d_out, int out_size, void* d_ws,
                              size_t ws_size, hipStream_t stream) {
  (void)in_sizes; (void)n_in; (void)out_size;
  if (ws_size < WS_END || ws_size < WS_CNT + (size_t)NH * CNT_STRIDE) return;
  fused_decode_kernel<<<NWG, NTHR, 0, stream>>>((const unsigned short*)d_in[0], (const char*)d_in[1], (const char*)d_in[2],
                                                (const char*)d_in[3], (const char*)d_in[4], (const char*)d_in[6],
                                                (const char*)d_in[7], (const char*)d_in[8], (unsigned short*)d_out, (char*)d_ws);
}
```

```cpp
#include <hip/hip_runtime.h>
#include <stdint.h>

typedef unsigned u32;
typedef unsigned long long u64;
typedef u32 u32x4 __attribute__((ext_vector_type(4)));
typedef u32 u32x2 __attribute__((ext_vector_type(2)));
typedef float f32x4 __attribute__((ext_vector_type(4)));
typedef float f32x2 __attribute__((ext_vector_type(2)));
typedef __bf16 bf16x2 __attribute__((ext_vector_type(2)));

constexpr int DM = 512, NH = 32, SK = 16384, DH = 128, FF = 1376;
constexpr int NW = 8, NTHR = NW * 64, NWG = 256;
constexpr int UKEYS = 16;
constexpr int UPB = SK / UKEYS;
constexpr int UEND = 2 * UPB;
constexpr int NSTAT = 4;
#define NL1 48
constexpr int NLOC = UPB / 8 + NL1;
constexpr int GLOB = UPB - 8 * NL1;
constexpr int NSLICE = 43, FS = 32;
constexpr int NRED = 64;
constexpr u32 SPIN_CAP = 1u << 20;
#define LD_WQ ld_nt
#define HOLD 100
#define POLL_AT 8

constexpr size_t WS_EPOCH = 0;
#define CNT_OFF 8388608
#define CNT_STRIDE 256
constexpr size_t WS_CNT = CNT_OFF;
constexpr size_t WS_Q = 16384;
constexpr size_t WS_ML = WS_Q + 2 * NH * 64 * 8;
constexpr size_t WS_P = WS_ML + NH * 2 * 8 * 16;
constexpr size_t WS_T = WS_P + (size_t)NH * 8 * 2 * 8 * 128;
constexpr size_t WS_F = WS_T + (size_t)NRED * NWG * 128;
constexpr size_t WS_END = WS_F + (size_t)NRED * NSLICE * 128;

constexpr int L_XN = 0;
constexpr int L_REC = L_XN + 2 * DM;
constexpr int L_MM = L_REC + 2 * NW * DH;
constexpr int L_LL = L_MM + 16;
constexpr int L_O = L_LL + 16;
constexpr int L_HS = L_O + 32;
constexpr int L_CNT = L_HS + 64;
constexpr int L_FR = L_CNT + 4;
constexpr int L_BIG = L_FR + 512;
constexpr int L_TK = L_BIG + 4 * 2 * DM;
constexpr int L_END = L_TK + 4;

__device__ __forceinline__ float bf16r(float x) { return (float)(__bf16)x; }
__device__ __forceinline__ float lo16(u32 w) { return __uint_as_float(w << 16); }
__device__ __forceinline__ float hi16(u32 w) { return __uint_as_float(w & 0xffff0000u); }
__device__ __forceinline__ float dot2(u32 a, u32 b, float c) {
  return __builtin_amdgcn_fdot2_f32_bf16(__builtin_bit_cast(bf16x2, a), __builtin_bit_cast(bf16x2, b), c, false);
}
template <int CTRL> __device__ __forceinline__ float dpp(float x) {
  return __builtin_bit_cast(float, __builtin_amdgcn_mov_dpp(__builtin_bit_cast(int, x), CTRL, 0xf, 0xf, true));
}
constexpr int XOR1 = 0xB1, XOR2 = 0x4E, XOR7 = 0x141, XOR8 = 0x128, ROR4 = 0x124;
__device__ __forceinline__ float row_sum16(float x) {
  x += dpp<XOR1>(x); x += dpp<XOR2>(x); x += dpp<XOR7>(x); x += dpp<XOR8>(x); return x;
}
__device__ __forceinline__ float row_sum_q4(float x) {
  x += dpp<ROR4>(x); x += dpp<XOR8>(x); return x;
}
__device__ __forceinline__ float xrow_max(float x) {
  auto s = __builtin_amdgcn_permlane16_swap(__float_as_uint(x), __float_as_uint(x), false, false);
  x = fmaxf(__uint_as_float(s[0]), __uint_as_float(s[1]));
  auto t = __builtin_amdgcn_permlane32_swap(__float_as_uint(x), __float_as_uint(x), false, false);
  return fmaxf(__uint_as_float(t[0]), __uint_as_float(t[1]));
}
__device__ __forceinline__ float xrow_sum(float x) {
  auto s = __builtin_amdgcn_permlane16_swap(__float_as_uint(x), __float_as_uint(x), false, false);
  x = __uint_as_float(s[0]) + __uint_as_float(s[1]);
  auto t = __builtin_amdgcn_permlane32_swap(__float_as_uint(x), __float_as_uint(x), false, false);
  return __uint_as_float(t[0]) + __uint_as_float(t[1]);
}
__device__ __forceinline__ float wave_sum(float x) { return xrow_sum(row_sum16(x)); }

#define STAMPW(i, w) do {} while (0)
#define STAMPU(u) do {} while (0)

struct Stage { u32x4 k[4]; u32x4 v[4]; };

#define STAGE_REGS(s) "+v"(s.k[0]), "+v"(s.k[1]), "+v"(s.k[2]), "+v"(s.k[3]), "+v"(s.v[0]), "+v"(s.v[1]), "+v"(s.v[2]), "+v"(s.v[3])
__device__ __forceinline__ void ld_plain(u32x4& d, u32 voff, const char* base) {
  asm volatile("s_nop 4\n\tglobal_load_dwordx4 %0, %1, %2" : "=&v"(d) : "v"(voff), "s"(base) : "memory");
}
__device__ __forceinline__ void ld_nt2(u32x2& d, u32 voff, const char* base) {
  asm volatile("s_nop 4\n\tglobal_load_dwordx2 %0, %1, %2 nt" : "=&v"(d) : "v"(voff), "s"(base) : "memory");
}
__device__ __forceinline__ void ld_nt(u32x4& d, u32 voff, const char* base) {
  asm volatile("s_nop 4\n\tglobal_load_dwordx4 %0, %1, %2 nt" : "=&v"(d) : "v"(voff), "s"(base) : "memory");
}
__device__ __forceinline__ void ld_dev(u32x4& d, u32 voff, const char* base) {
  asm volatile("s_nop 4\n\tglobal_load_dwordx4 %0, %1, %2 sc1" : "=&v"(d) : "v"(voff), "s"(base) : "memory");
}
__device__ __forceinline__ void st_dev(u32 voff, char* base, float a, float b, u32 tag) {
  const u32x4 d = {__float_as_uint(a), tag, __float_as_uint(b), tag};
  asm volatile("s_nop 4\n\tglobal_store_dwordx4 %0, %1, %2 sc1" :: "v"(voff), "v"(d), "s"(base) : "memory");
}
__device__ __forceinline__ void ticket(u32& t, const char* cnt) {
  asm volatile("s_nop 4\n\ts_mov_b64 exec, 1\n\tglobal_atomic_add %0, %1, %2, %3 sc0\n\ts_mov_b64 exec, -1"
               : "=&v"(t) : "v"(0u), "v"(1u), "s"(cnt) : "memory");
}
template <int N> __device__ __forceinline__ u32 read_ticket(u32 t) {
  u32 v;
  asm volatile("s_waitcnt vmcnt(%2)\n\tv_readfirstlane_b32 %0, %1\n\ts_nop 4" : "=s"(v) : "v"(t), "i"(N) : "memory");
  return v;
}
__device__ __forceinline__ void issue(Stage& s, u32 voff, const char* kb, const char* vb) {
  asm volatile("s_nop 4\n\t"
               "global_load_dwordx4 %0, %8, %9 nt\n\t"
               "global_load_dwordx4 %1, %8, %9 offset:1024 nt\n\t"
               "global_load_dwordx4 %2, %8, %9 offset:2048 nt\n\t"
               "global_load_dwordx4 %3, %8, %9 offset:3072 nt\n\t"
               "global_load_dwordx4 %4, %8, %10 nt\n\t"
               "global_load_dwordx4 %5, %8, %10 offset:1024 nt\n\t"
               "global_load_dwordx4 %6, %8, %10 offset:2048 nt\n\t"
               "global_load_dwordx4 %7, %8, %10 offset:3072 nt"
               : "=&v"(s.k[0]), "=&v"(s.k[1]), "=&v"(s.k[2]), "=&v"(s.k[3]), "=&v"(s.v[0]), "=&v"(s.v[1]), "=&v"(s.v[2]), "=&v"(s.v[3])
               : "v"(voff), "s"(kb), "s"(vb) : "memory");
}
__device__ __forceinline__ void refill(Stage& s, u32& t, u32 voff, const char* kb, const char* vb, const char* cnt, int u) {
  asm volatile("s_nop 4\n\t"
               "s_cmpk_lt_i32 %15, 0x800\n\t"
               "s_cbranch_scc0 1f\n\t"
               "global_load_dwordx4 %0, %9, %10 nt\n\t"
               "global_load_dwordx4 %1, %9, %10 offset:1024 nt\n\t"
               "global_load_dwordx4 %2, %9, %10 offset:2048 nt\n\t"
               "global_load_dwordx4 %3, %9, %10 offset:3072 nt\n\t"
               "global_load_dwordx4 %4, %9, %11 nt\n\t"
               "global_load_dwordx4 %5, %9, %11 offset:1024 nt\n\t"
               "global_load_dwordx4 %6, %9, %11 offset:2048 nt\n\t"
               "global_load_dwordx4 %7, %9, %11 offset:3072 nt\n\t"
               "s_mov_b64 exec, 1\n\t"
               "global_atomic_add %8, %13, %14, %12 sc0\n\t"
               "s_mov_b64 exec, -1\n\t"
               "s_branch 2f\n"
               "1:\n\t"
               "s_waitcnt vmcnt(0)\n"
               "2:"
               : STAGE_REGS(s), "=&v"(t) : "v"(voff), "s"(kb), "s"(vb), "s"(cnt), "v"(0u), "v"(1u), "s"(u) : "memory", "scc");
}
__device__ __forceinline__ void refill_own(Stage& s, u32& t, u32 voff, const char* kb, const char* vb, const char* cnt, int g) {
  asm volatile("s_nop 4\n\t"
               "global_load_dwordx4 %0, %9, %10 nt\n\t"
               "global_load_dwordx4 %1, %9, %10 offset:1024 nt\n\t"
               "global_load_dwordx4 %2, %9, %10 offset:2048 nt\n\t"
               "global_load_dwordx4 %3, %9, %10 offset:3072 nt\n\t"
               "global_load_dwordx4 %4, %9, %11 nt\n\t"
               "global_load_dwordx4 %5, %9, %11 offset:1024 nt\n\t"
               "global_load_dwordx4 %6, %9, %11 offset:2048 nt\n\t"
               "global_load_dwordx4 %7, %9, %11 offset:3072 nt\n\t"
               "s_cmpk_lt_i32 %15, %16\n\t"
               "s_cbranch_scc1 1f\n\t"
               "s_mov_b64 exec, 1\n\t"
               "global_atomic_add %8, %13, %14, %12 sc0\n\t"
               "s_mov_b64 exec, -1\n"
               "1:"
               : STAGE_REGS(s), "+v"(t) : "v"(voff), "s"(kb), "s"(vb), "s"(cnt), "v"(0u), "v"(1u), "s"(g), "i"(NLOC - 2 * NW) : "memory", "scc");
}
static_assert(UEND == 0x800, "refill compares against 0x800");
template <int N> __device__ __forceinline__ void wait_k(Stage& s) {
  asm volatile("s_waitcnt vmcnt(%4)" : "+v"(s.k[0]), "+v"(s.k[1]), "+v"(s.k[2]), "+v"(s.k[3]) : "i"(N) : "memory");
}
template <int N> __device__ __forceinline__ void wait_v(Stage& s) {
  asm volatile("s_waitcnt vmcnt(%4)" : "+v"(s.v[0]), "+v"(s.v[1]), "+v"(s.v[2]), "+v"(s.v[3]) : "i"(N) : "memory");
}
template <int N, typename V> __device__ __forceinline__ void wait_1(V& d) { asm volatile("s_waitcnt vmcnt(%1)" : "+v"(d) : "i"(N) : "memory"); }
template <int N> __device__ __forceinline__ void wait_n() { asm volatile("s_waitcnt vmcnt(%0)" :: "i"(N) : "memory"); }

template <int LATER>
__device__ __forceinline__ void consume(Stage& s, const u32x4 q, float& m, float& l, float (&acc)[8]) {
  constexpr float SCALE = 0.08838834764831845f, LOG2E = 1.4426950408889634f;
  float y[4];
  wait_k<LATER + 4>(s);
#pragma unroll
  for (int t = 0; t < 4; ++t) {
    float d = dot2(s.k[t][0], q[0], 0.f);
    d = dot2(s.k[t][1], q[1], d); d = dot2(s.k[t][2], q[2], d); d = dot2(s.k[t][3], q[3], d);
    d = row_sum16(d);
    y[t] = bf16r(bf16r(d) * SCALE) * LOG2E;
  }
  float mn = m;
#pragma unroll
  for (int t = 0; t < 4; ++t) mn = fmaxf(mn, y[t]);
  const float alpha = __builtin_amdgcn_exp2f(m - mn);
  float ps = 0.f;
#pragma unroll
  for (int e = 0; e < 8; ++e) acc[e] *= alpha;
  wait_v<LATER>(s);
#pragma unroll
  for (int t = 0; t < 4; ++t) {
    const float p = __builtin_amdgcn_exp2f(y[t] - mn);
    ps += p;
#pragma unroll
    for (int e = 0; e < 4; ++e) {
      acc[2 * e] = fmaf(p, lo16(s.v[t][e]), acc[2 * e]);
      acc[2 * e + 1] = fmaf(p, hi16(s.v[t][e]), acc[2 * e + 1]);
    }
  }
  l = fmaf(l, alpha, ps);
  m = mn;
  asm volatile("" : "+v"(acc[0]), "+v"(acc[1]), "+v"(acc[2]), "+v"(acc[3]), "+v"(acc[4]), "+v"(acc[5]), "+v"(acc[6]), "+v"(acc[7]), "+v"(l), "+v"(m));
  __builtin_amdgcn_sched_barrier(0);
}

constexpr float M_NONE = -1.0e30f;

__device__ __forceinline__ u32 lds_inc(float* lds, int idx) {
  return __hip_atomic_fetch_add(&reinterpret_cast<u32*>(lds)[idx], 1u, __ATOMIC_RELAXED, __HIP_MEMORY_SCOPE_WORKGROUP);
}

__device__ __forceinline__ void layer_norm(const u32x4 (&xr)[2], float* lds, int lane) {
#pragma unroll
  for (int bb = 0; bb < 2; ++bb) {
    float xv[8];
#pragma unroll
    for (int e = 0; e < 4; ++e) { xv[2 * e] = lo16(xr[bb][e]); xv[2 * e + 1] = hi16(xr[bb][e]); }
    float s = 0.f;
#pragma unroll
    for (int e = 0; e < 8; ++e) s += xv[e];
    const float mean = wave_sum(s) * (1.f / DM);
    float s2 = 0.f;
#pragma unroll
    for (int e = 0; e < 8; ++e) { const float d = xv[e] - mean; s2 = fmaf(d, d, s2); }
    const float var = bf16r(wave_sum(s2) * (1.f / DM));
    const float mb = bf16r(mean);
    const float den = bf16r(sqrtf(bf16r(var + 1e-5f)));
    f32x4 o0, o1;
#pragma unroll
    for (int e = 0; e < 4; ++e) { o0[e] = bf16r(bf16r(xv[e] - mb) / den); o1[e] = bf16r(bf16r(xv[4 + e] - mb) / den); }
    *reinterpret_cast<f32x4*>(&lds[L_XN + bb * DM + lane * 8]) = o0;
    *reinterpret_cast<f32x4*>(&lds[L_XN + bb * DM + lane * 8 + 4]) = o1;
  }
  asm volatile("s_waitcnt lgkmcnt(0)" ::: "memory");
}

__global__ void __launch_bounds__(NTHR)
fused_decode_kernel(const unsigned short* __restrict__ x, const char* __restrict__ kc, const char* __restrict__ vc,
                    const char* __restrict__ wq, const char* __restrict__ wo, const char* __restrict__ wg,
                    const char* __restrict__ wu, const char* __restrict__ wd, unsigned short* __restrict__ out,
                    char* __restrict__ ws) {
  __shared__ __attribute__((aligned(16))) float lds[L_END];
  const int tid = threadIdx.x, lane = tid & 63, wave = __builtin_amdgcn_readfirstlane(tid >> 6);
  const int r = lane >> 4, c = lane & 15;
  const int id = blockIdx.x, i = id & 7, h = id >> 3;
  int slice = -1;
  if (i == ((h >> 1) & 7)) slice = h;
  else if (h % 3 == 0 && i == (((h / 6) * 3 + 5) & 7)) slice = 32 + h / 3;
  const int dd = ((h >> 1) - i) & 7;
  const bool is_q = (dd & 3) == 2;
  const bool ffn_wave = slice >= 0 && wave >= 1 && wave <= 4;
  const bool is_red = (i & 3) == (h & 3);
  const int red = 2 * h + (i >> 2);
  const u32 voff = lane * 16;
  const u32 tag = *(const u32*)(ws + WS_EPOCH) + 1u;
  const u32 ctag = (tag ^ 0x80000u) & 0xfffffu;
  const char* cnt = ws + WS_CNT + (size_t)h * CNT_STRIDE;
  STAMPW(0, 0);
  if (tid < 4) reinterpret_cast<u32*>(lds)[L_CNT + tid] = 0u;
  if (tid == 4) reinterpret_cast<u32*>(lds)[L_TK] = (u32)(NW * (NSTAT + 2));
  __syncthreads();

  if (i == 0 && tid == 0)
    __hip_atomic_store((__attribute__((address_space(1))) u32*)(ws + WS_CNT + (size_t)h * CNT_STRIDE), ctag << 12,
                       __ATOMIC_RELAXED, __HIP_MEMORY_SCOPE_AGENT);
  u32x2 wor[4];
  auto load_wo = [&]() {
#pragma unroll
    for (int k = 0; k < 4; ++k) ld_nt2(wor[k], (u32)((4 * r + k) * (DM * 2) + c * 8), wo + (size_t)(h * DH + 16 * i) * (DM * 2) + wave * 128);
  };
  load_wo();
  if (!is_q && !ffn_wave) {
    const u64 t_start = __builtin_amdgcn_s_memrealtime();
    const size_t hb = (size_t)h * SK * DH * 2 + (size_t)(i * (UPB / 8) + wave) * (UKEYS * DH * 2);
    u32 t0, t1, t2;
    asm volatile("global_load_dword %0, %3, %4 nt\n\tglobal_load_dword %1, %3, %5 nt\n\tglobal_load_dword %2, %3, %6 sc1"
                 : "=&v"(t0), "=&v"(t1), "=&v"(t2) : "v"(0u), "s"(kc + hb), "s"(vc + hb), "s"(ws + WS_Q + h * 512) : "memory");
    for (int spins = 0; spins < 4096 && __builtin_amdgcn_s_memrealtime() - t_start < HOLD; ++spins) __builtin_amdgcn_s_sleep(2);
    asm volatile("s_waitcnt vmcnt(0)" : "+v"(t0), "+v"(t1), "+v"(t2) :: "memory");
  }
  if (is_q) {
    const int qi = 2 * h + (dd >> 2);
    u32x4 xr[2], wr[8];
    ld_plain(xr[0], voff, (const char*)x);
    ld_plain(xr[1], voff, (const char*)x + 1024);
    {
      const u32 vo = (lane >> 3) * (NH * DH * 2) + (lane & 7) * 16;
#pragma unroll
      for (int k = 0; k < 8; ++k) LD_WQ(wr[k], vo, wq + qi * 128 + (size_t)(64 * wave + 8 * k) * (NH * DH * 2));
    }
    wait_1<8>(xr[0]); wait_1<8>(xr[1]);
    layer_norm(xr, lds, lane);
    float qa[2][8];
#pragma unroll
    for (int e = 0; e < 8; ++e) { qa[0][e] = 0.f; qa[1][e] = 0.f; }
    for (int z = 0; z < 8; ++z) wait_1<0>(wr[z]);
#pragma unroll
    for (int k = 0; k < 8; ++k) {
      const float x0 = lds[L_XN + 64 * wave + (lane >> 3) + 8 * k], x1 = lds[L_XN + DM + 64 * wave + (lane >> 3) + 8 * k];
#pragma unroll
      for (int e = 0; e < 4; ++e) {
        const float a = lo16(wr[k][e]), bq = hi16(wr[k][e]);
        qa[0][2 * e] = fmaf(x0, a, qa[0][2 * e]); qa[0][2 * e + 1] = fmaf(x0, bq, qa[0][2 * e + 1]);
        qa[1][2 * e] = fmaf(x1, a, qa[1][2 * e]); qa[1][2 * e + 1] = fmaf(x1, bq, qa[1][2 * e + 1]);
      }
    }
#pragma unroll
    for (int bb = 0; bb < 2; ++bb) {
#pragma unroll
      for (int e = 0; e < 8; ++e) { const float t = qa[bb][e] + dpp<XOR8>(qa[bb][e]); qa[bb][e] = xrow_sum(t); }
      if (lane < 8) {
        float* rec = &lds[L_BIG + (wave * 2 + bb) * 64 + lane * 8];
        *reinterpret_cast<f32x4*>(rec) = f32x4{qa[bb][0], qa[bb][1], qa[bb][2], qa[bb][3]};
        *reinterpret_cast<f32x4*>(rec + 4) = f32x4{qa[bb][4], qa[bb][5], qa[bb][6], qa[bb][7]};
      }
    }
    asm volatile("s_waitcnt lgkmcnt(0)" ::: "memory");
    u32 arr = 0;
    if (lane == 0) arr = lds_inc(lds, L_CNT + 3);
    if (__builtin_amdgcn_readfirstlane(arr) == NW - 1) {
      asm volatile("" ::: "memory");
      if (lane < 32) {
        f32x4 t = {0.f, 0.f, 0.f, 0.f};
#pragma unroll
        for (int w = 0; w < NW; ++w) t += *reinterpret_cast<const f32x4*>(&lds[L_BIG + w * 128 + lane * 4]);
        const bf16x2 p0 = {(__bf16)t[0], (__bf16)t[1]}, p1 = {(__bf16)t[2], (__bf16)t[3]};
        st_dev((u32)(((lane >> 4) * NH * 64 + qi * 32 + (lane & 15) * 2) * 8), ws + WS_Q,
               __builtin_bit_cast(float, p0), __builtin_bit_cast(float, p1), tag);
      }
      STAMPW(7, wave);
    }
  } else if (ffn_wave) {
    const int fw = wave - 1, ft = tid - 64;
    u32x4 xr[2], gw[8], uw[8], dw[8];
    ld_plain(xr[0], voff, (const char*)x);
    ld_plain(xr[1], voff, (const char*)x + 1024);
    {
      const u32 vo = (u32)((ft >> 2) * (FF * 2) + (ft & 3) * 16);
#pragma unroll
      for (int k = 0; k < 8; ++k) ld_nt(gw[k], vo, wg + slice * (FS * 2) + (size_t)k * 64 * (FF * 2));
#pragma unroll
      for (int k = 0; k < 8; ++k) ld_nt(uw[k], vo, wu + slice * (FS * 2) + (size_t)k * 64 * (FF * 2));
#pragma unroll
      for (int k = 0; k < 8; ++k) ld_nt(dw[k], voff, wd + (size_t)(slice * FS + 8 * fw + k) * (DM * 2));
    }
    wait_1<24>(xr[0]); wait_1<24>(xr[1]);
    layer_norm(xr, lds, lane);
    for (int z = 0; z < 8; ++z) { wait_1<8>(gw[z]); wait_1<8>(uw[z]); }
#pragma unroll
    for (int bb = 0; bb < 2; ++bb) {
      float ga[8], ua[8];
#pragma unroll
      for (int e = 0; e < 8; ++e) { ga[e] = 0.f; ua[e] = 0.f; }
#pragma unroll
      for (int k = 0; k < 8; ++k) {
        const float xv = lds[L_XN + bb * DM + (ft >> 2) + 64 * k];
#pragma unroll
        for (int e = 0; e < 4; ++e) {
          ga[2 * e] = fmaf(xv, lo16(gw[k][e]), ga[2 * e]); ga[2 * e + 1] = fmaf(xv, hi16(gw[k][e]), ga[2 * e + 1]);
          ua[2 * e] = fmaf(xv, lo16(uw[k][e]), ua[2 * e]); ua[2 * e + 1] = fmaf(xv, hi16(uw[k][e]), ua[2 * e + 1]);
        }
      }
#pragma unroll
      for (int e = 0; e < 8; ++e) { ga[e] = xrow_sum(row_sum_q4(ga[e])); ua[e] = xrow_sum(row_sum_q4(ua[e])); }
      if (lane < 4) {
        float* rec = &lds[L_FR + fw * 128 + bb * 64 + lane * 8];
        *reinterpret_cast<f32x4*>(rec) = f32x4{ga[0], ga[1], ga[2], ga[3]};
        *reinterpret_cast<f32x4*>(rec + 4) = f32x4{ga[4], ga[5], ga[6], ga[7]};
        *reinterpret_cast<f32x4*>(rec + 32) = f32x4{ua[0], ua[1], ua[2], ua[3]};
        *reinterpret_cast<f32x4*>(rec + 36) = f32x4{ua[4], ua[5], ua[6], ua[7]};
      }
    }
    asm volatile("s_waitcnt lgkmcnt(0)" ::: "memory");
    u32 arr = 0;
    if (lane == 0) arr = lds_inc(lds, L_CNT + 0);
    if (__builtin_amdgcn_readfirstlane(arr) == 3) {
      asm volatile("" ::: "memory");
      const int bb = lane >> 5, col = lane & 31;
      float g = 0.f, u = 0.f;
#pragma unroll
      for (int w = 0; w < 4; ++w) { g += lds[L_FR + w * 128 + bb * 64 + col]; u += lds[L_FR + w * 128 + bb * 64 + 32 + col]; }
      g = bf16r(g); u = bf16r(u);
      const float inner = 0.7978845608028654f * (g + 0.044715f * g * g * g);
      const float ge = bf16r(0.5f * g * (1.f + tanhf(inner)));
      lds[L_HS + lane] = bf16r(ge * u);
      asm volatile("s_waitcnt lgkmcnt(0)" ::: "memory");
      if (lane == 0) __hip_atomic_store(&reinterpret_cast<u32*>(lds)[L_CNT + 1], 1u, __ATOMIC_RELAXED, __HIP_MEMORY_SCOPE_WORKGROUP);
    }
    for (u32 spins = 0; spins < SPIN_CAP; ++spins) {
      if (__hip_atomic_load(&reinterpret_cast<u32*>(lds)[L_CNT + 1], __ATOMIC_RELAXED, __HIP_MEMORY_SCOPE_WORKGROUP) != 0u) break;
      __builtin_amdgcn_s_sleep(1);
    }
    asm volatile("" ::: "memory");
    for (int z = 0; z < 8; ++z) wait_1<0>(dw[z]);
#pragma unroll
    for (int bb = 0; bb < 2; ++bb) {
      float da[8];
#pragma unroll
      for (int e = 0; e < 8; ++e) da[e] = 0.f;
#pragma unroll
      for (int k = 0; k < 8; ++k) {
        const float hv = lds[L_HS + bb * 32 + 8 * fw + k];
#pragma unroll
        for (int e = 0; e < 4; ++e) { da[2 * e] = fmaf(hv, lo16(dw[k][e]), da[2 * e]); da[2 * e + 1] = fmaf(hv, hi16(dw[k][e]), da[2 * e + 1]); }
      }
      float* rec = &lds[L_BIG + (fw * 2 + bb) * DM + lane * 8];
      *reinterpret_cast<f32x4*>(rec) = f32x4{da[0], da[1], da[2], da[3]};
      *reinterpret_cast<f32x4*>(rec + 4) = f32x4{da[4], da[5], da[6], da[7]};
    }
    asm volatile("s_waitcnt lgkmcnt(0)" ::: "memory");
    if (lane == 0) arr = lds_inc(lds, L_CNT + 2);
    if (__builtin_amdgcn_readfirstlane(arr) == 3) {
      asm volatile("" ::: "memory");
      const int pc = lane & 7;
#pragma unroll
      for (int k = 0; k < 8; ++k) {
        const int rd = 8 * k + (lane >> 3);
        const int idx = (pc >> 2) * DM + rd * 8 + 2 * (pc & 3);
        float s0 = 0.f, s1 = 0.f;
#pragma unroll
        for (int w = 0; w < 4; ++w) { const f32x2 t2 = *reinterpret_cast<const f32x2*>(&lds[L_BIG + w * 2 * DM + idx]); s0 += t2[0]; s1 += t2[1]; }
        st_dev((u32)((rd * NSLICE + slice) * 128 + pc * 16), ws + WS_F, s0, s1, tag);
      }
    }
    STAMPW(2, 1);
  }

  constexpr size_t UB = (size_t)UKEYS * DH * 2;
  const char* kp = kc + (size_t)h * SK * DH * 2;
  const char* vp = vc + (size_t)h * SK * DH * 2;
  auto uoff = [&](int u) -> size_t {
    u = min(u, UEND - 1);
    return (size_t)(u & (UPB - 1)) * UB + (size_t)(u >> 10) * ((size_t)NH * SK * DH * 2);
  };
  Stage A, B, C, D;
  const int us = i * (UPB / 8) + wave;
  issue(A, voff, kp + us * UB, vp + us * UB);
  issue(B, voff, kp + (NW + us) * UB, vp + (NW + us) * UB);
  issue(C, voff, kp + (2 * NW + us) * UB, vp + (2 * NW + us) * UB);
  STAMPW(24 + wave, wave);
  wait_n<POLL_AT>();
  u32 tA = 0, tB = 0;
  u32x4 q0, q1;
  {
    u32x4 g[4];
    const u32 qo = (u32)((h * 64 + 4 * c) * 8);
    ld_dev(g[0], qo, ws + WS_Q); ld_dev(g[1], qo + 16, ws + WS_Q);
    ld_dev(g[2], qo, ws + WS_Q + NH * 64 * 8); ld_dev(g[3], qo + 16, ws + WS_Q + NH * 64 * 8);
    issue(D, voff, kp + (3 * NW + us) * UB, vp + (3 * NW + us) * UB);
    wait_1<8>(g[0]); wait_1<8>(g[1]); wait_1<8>(g[2]); wait_1<8>(g[3]);
    for (u32 spins = 0; spins < SPIN_CAP; ++spins) {
      bool ok = true;
#pragma unroll
      for (int k = 0; k < 4; ++k) ok = ok && g[k][1] == tag && g[k][3] == tag;
      if (__all(ok)) break;
      __builtin_amdgcn_s_sleep(2);
      ld_dev(g[0], qo, ws + WS_Q); ld_dev(g[1], qo + 16, ws + WS_Q);
      ld_dev(g[2], qo, ws + WS_Q + NH * 64 * 8); ld_dev(g[3], qo + 16, ws + WS_Q + NH * 64 * 8);
      wait_1<0>(g[0]); wait_1<0>(g[1]); wait_1<0>(g[2]); wait_1<0>(g[3]);
    }
    q0 = u32x4{g[0][0], g[0][2], g[1][0], g[1][2]};
    q1 = u32x4{g[2][0], g[2][2], g[3][0], g[3][2]};
  }
  STAMPW(8 + wave, wave);
  float m = M_NONE, l = 0.f, acc[8];
#pragma unroll
  for (int e = 0; e < 8; ++e) acc[e] = 0.f;
  auto next_unit = [&](u32 val) -> int {
    for (u32 spins = 0; (val >> 12) != ctag; ++spins) {
      if (spins >= SPIN_CAP) return UEND;
      u32 t2;
      ticket(t2, cnt); val = read_ticket<0>(t2);
    }
    return (val & 0xfffu) < (u32)GLOB ? UPB + (int)(val & 0xfffu) : UEND;
  };
  auto flush = [&](int bb) {
    const float mw = xrow_max(m);
    const float sc = __builtin_amdgcn_exp2f(m - mw);
    const float lw = xrow_sum(l * sc);
    float a[8];
#pragma unroll
    for (int e = 0; e < 8; ++e) a[e] = xrow_sum(acc[e] * sc);
    if (r == 0) {
      float* rec = &lds[L_REC + (bb * NW + wave) * DH + 8 * c];
      *reinterpret_cast<f32x4*>(rec) = f32x4{a[0], a[1], a[2], a[3]};
      *reinterpret_cast<f32x4*>(rec + 4) = f32x4{a[4], a[5], a[6], a[7]};
      if (c == 0) { lds[L_MM + bb * NW + wave] = mw; lds[L_LL + bb * NW + wave] = lw; }
    }
    m = M_NONE; l = 0.f;
#pragma unroll
    for (int e = 0; e < 8; ++e) acc[e] = 0.f;
  };
  int uA, uB;
  for (int k = 0; k < 4; ++k) wait_1<8>(wor[k]);
  auto draw = [&]() -> u32 { u32 n = 0; if (lane == 0) n = lds_inc(lds, L_TK); return n; };
  auto local_unit = [&](int n) -> int { return n < UPB / 8 ? i * (UPB / 8) + n : UPB + GLOB + NL1 * i + (n - UPB / 8); };
  uA = us + 4 * NW; uB = us + 5 * NW;
  consume<8>(A, q0, m, l, acc); issue(A, voff, kp + uA * UB, vp + uA * UB);
  consume<8>(B, q0, m, l, acc); issue(B, voff, kp + uB * UB, vp + uB * UB);
  consume<16>(C, q0, m, l, acc);
  consume<16>(D, q0, m, l, acc);
  u32x4 q = q0;
  int curb = 0;
  static_assert(NLOC - 2 * NW >= NW * (NSTAT + 2), "window");
  bool gB = false, skipA = false;
  for (;;) {
    const u32 dA = draw();
    if (uA >= UPB && curb == 0) { flush(0); q = q1; curb = 1; }
    consume<8>(A, q, m, l, acc);
    const int nA = __builtin_amdgcn_readfirstlane(dA);
    uA = local_unit(nA);
    const bool gA = nA >= NLOC - 2 * NW;
    refill_own(A, tA, voff, kp + uoff(uA), vp + uoff(uA), cnt, nA);
    if (gB) { skipA = true; break; }
    const u32 dB = draw();
    if (uB >= UPB && curb == 0) { flush(0); q = q1; curb = 1; }
    consume<8>(B, q, m, l, acc);
    const int nB = __builtin_amdgcn_readfirstlane(dB);
    uB = local_unit(nB);
    gB = nB >= NLOC - 2 * NW;
    refill_own(B, tB, voff, kp + uoff(uB), vp + uoff(uB), cnt, nB);
    if (gA) break;
  }
  int fails = (uA >= UEND ? 1 : 0) + (uB >= UEND ? 1 : 0);
  u32 warm = 0;
  if (wave == 0) {
    const u32 wo_ = lane < 32 ? (u32)WS_T + (u32)((2 * lane * NWG + id) * 128)
                  : lane < 40 ? (u32)WS_P + (u32)(h * 16384 + (lane - 32) * 2048)
                  : lane < 42 ? (u32)WS_F + (u32)(red * (NSLICE * 128) + (lane - 40) * 4096) : (u32)WS_ML + (u32)(h * 256);
    asm volatile("global_load_dword %0, %1, %2 sc1" : "+v"(warm) : "v"(wo_), "s"(ws) : "memory");
  }
  while (fails < 2) {
    if (uA < UEND && !skipA) {
      if (fails) wait_n<0>();
      if (uA >= UPB && curb == 0) { flush(0); q = q1; curb = 1; }
      consume<10>(A, q, m, l, acc);
      uA = next_unit(read_ticket<9>(tA)); refill(A, tA, voff, kp + uoff(uA), vp + uoff(uA), cnt, uA);
      STAMPU(uA);
      if (uA >= UEND) ++fails;
    }
    skipA = false;
    if (uB < UEND) {
      if (fails) wait_n<0>();
      if (uB >= UPB && curb == 0) { flush(0); q = q1; curb = 1; }
      consume<10>(B, q, m, l, acc);
      uB = next_unit(read_ticket<9>(tB)); refill(B, tB, voff, kp + uoff(uB), vp + uoff(uB), cnt, uB);
      STAMPU(uB);
      if (uB >= UEND) ++fails;
    }
  }
  STAMPW(16 + wave, wave);
  asm volatile("s_waitcnt vmcnt(0)" : "+v"(warm) :: "memory");
  if (curb == 0) flush(0);
  flush(1);
  __syncthreads();

  if (wave < 2) {
    const int bb = 1 - wave;
    const f32x4 m0 = *reinterpret_cast<const f32x4*>(&lds[L_MM + bb * NW]), m1 = *reinterpret_cast<const f32x4*>(&lds[L_MM + bb * NW + 4]);
    const f32x4 l0 = *reinterpret_cast<const f32x4*>(&lds[L_LL + bb * NW]), l1 = *reinterpret_cast<const f32x4*>(&lds[L_LL + bb * NW + 4]);
    const float M = fmaxf(fmaxf(fmaxf(m0[0], m0[1]), fmaxf(m0[2], m0[3])), fmaxf(fmaxf(m1[0], m1[1]), fmaxf(m1[2], m1[3])));
    float L = 0.f, a0 = 0.f, a1 = 0.f;
#pragma unroll
    for (int w = 0; w < NW; ++w) {
      const float wi = __builtin_amdgcn_exp2f((w < 4 ? m0[w & 3] : m1[w & 3]) - M);
      L = fmaf(wi, w < 4 ? l0[w & 3] : l1[w & 3], L);
      const f32x2 a2 = *reinterpret_cast<const f32x2*>(&lds[L_REC + (bb * NW + w) * DH + 2 * lane]);
      a0 = fmaf(wi, a2[0], a0); a1 = fmaf(wi, a2[1], a1);
    }
    st_dev((u32)(((((lane >> 3) * 2 + bb) * 8 + i) * 16 + 2 * (lane & 7)) * 8), ws + WS_P + (size_t)h * (8 * 2 * 8 * 128), a0, a1, tag);
    if (lane == 0) st_dev((u32)(((h * 2 + bb) * 8 + i) * 16), ws + WS_ML, M, L, tag);
  }
  STAMPW(4, 0);
  if (wave == 0) {
    const int bb = (lane >> 3) & 1, j = lane & 7;
    const u32 po = (u32)(((bb * 8 + j) * 16 + 4 * r) * 8), mo = (u32)(((h * 2 + bb) * 8 + j) * 16);
    const char* pb = ws + WS_P + (size_t)(h * 8 + i) * (2 * 8 * 128);
    u32x4 g0, g1, gm;
    for (u32 spins = 0; spins < SPIN_CAP; ++spins) {
      ld_dev(g0, po, pb); ld_dev(g1, po + 16, pb); ld_dev(gm, mo, ws + WS_ML);
      wait_1<0>(g0); wait_1<0>(g1); wait_1<0>(gm);
      const bool ok = g0[1] == tag && g0[3] == tag && g1[1] == tag && g1[3] == tag && gm[1] == tag && gm[3] == tag;
      if (__all(ok)) break;
      __builtin_amdgcn_s_sleep(1);
    }
    const float mj = __uint_as_float(gm[0]), lj = __uint_as_float(gm[2]);
    float Mh = fmaxf(mj, dpp<XOR1>(mj)); Mh = fmaxf(Mh, dpp<XOR2>(Mh)); Mh = fmaxf(Mh, dpp<XOR7>(Mh));
    const float wj = __builtin_amdgcn_exp2f(mj - Mh);
    float v[5] = {wj * lj, wj * __uint_as_float(g0[0]), wj * __uint_as_float(g0[2]), wj * __uint_as_float(g1[0]), wj * __uint_as_float(g1[2])};
#pragma unroll
    for (int e = 0; e < 5; ++e) { v[e] += dpp<XOR1>(v[e]); v[e] += dpp<XOR2>(v[e]); v[e] += dpp<XOR7>(v[e]); }
    if (j == 0) *reinterpret_cast<f32x4*>(&lds[L_O + bb * 16 + 4 * r]) = f32x4{bf16r(v[1] / v[0]), bf16r(v[2] / v[0]), bf16r(v[3] / v[0]), bf16r(v[4] / v[0])};
  } else if (is_red && wave >= 2) {
    const int e = tid - 128;
    if (e < NSLICE * 8) {
      u32x4 g;
      for (u32 spins = 0; spins < SPIN_CAP; ++spins) {
        ld_dev(g, (u32)(e * 16), ws + WS_F + (size_t)red * (NSLICE * 128));
        wait_1<0>(g);
        if (__all(g[1] == tag && g[3] == tag)) break;
        __builtin_amdgcn_s_sleep(4);
      }
      *reinterpret_cast<f32x2*>(&lds[L_BIG + 2 * e]) = f32x2{__uint_as_float(g[0]), __uint_as_float(g[2])};
    }
  }
  __syncthreads();
  STAMPW(5, 0);
  {
    const f32x4 o0 = *reinterpret_cast<const f32x4*>(&lds[L_O + 4 * r]), o1 = *reinterpret_cast<const f32x4*>(&lds[L_O + 16 + 4 * r]);
    float t0[4], t1[4];
#pragma unroll
    for (int e = 0; e < 4; ++e) { t0[e] = 0.f; t1[e] = 0.f; }
#pragma unroll
    for (int k = 0; k < 4; ++k) {
      const float w4[4] = {lo16(wor[k][0]), hi16(wor[k][0]), lo16(wor[k][1]), hi16(wor[k][1])};
#pragma unroll
      for (int e = 0; e < 4; ++e) { t0[e] = fmaf(o0[k], w4[e], t0[e]); t1[e] = fmaf(o1[k], w4[e], t1[e]); }
    }
#pragma unroll
    for (int e = 0; e < 4; ++e) { t0[e] = xrow_sum(t0[e]); t1[e] = xrow_sum(t1[e]); }
    const float sa = r == 0 ? t0[0] : r == 1 ? t0[2] : r == 2 ? t1[0] : t1[2];
    const float sb = r == 0 ? t0[1] : r == 1 ? t0[3] : r == 2 ? t1[1] : t1[3];
    st_dev((u32)(((8 * wave + (c >> 1)) * NWG + id) * 128 + (r >> 1) * 64 + ((c & 1) * 4 + (r & 1) * 2) * 8), ws + WS_T, sa, sb, tag);
  }
  if (!is_red) return;

  float gate = 0.f;
  if (tid < 16) {
    float d = 0.f;
#pragma unroll
    for (int s = 0; s < NSLICE; ++s) d += lds[L_BIG + s * 16 + tid];
    d = bf16r(d);
    gate = bf16r(d / (1.f + __expf(-d)));
  }
  {
    const char* tb = ws + WS_T + (size_t)red * (NWG * 128);
    u32x4 g[4];
    for (u32 spins = 0; spins < SPIN_CAP; ++spins) {
#pragma unroll
      for (int k = 0; k < 4; ++k) ld_dev(g[k], (u32)(tid * 16), tb + k * 8192);
      bool ok = true;
#pragma unroll
      for (int k = 0; k < 4; ++k) { wait_1<0>(g[k]); ok = ok && g[k][1] == tag && g[k][3] == tag; }
      if (__all(ok)) break;
      __builtin_amdgcn_s_sleep(1);
    }
    float s0 = 0.f, s1 = 0.f;
#pragma unroll
    for (int k = 0; k < 4; ++k) { s0 += __uint_as_float(g[k][0]); s1 += __uint_as_float(g[k][2]); }
    s0 += dpp<XOR8>(s0); s1 += dpp<XOR8>(s1);
    s0 = xrow_sum(s0); s1 = xrow_sum(s1);
    if (lane < 8) *reinterpret_cast<f32x2*>(&lds[L_FR + wave * 16 + 2 * lane]) = f32x2{s0, s1};
  }
  __syncthreads();
  if (tid < 16) {
    float s = 0.f;
#pragma unroll
    for (int w = 0; w < NW; ++w) s += lds[L_FR + w * 16 + tid];
    const int o = (tid >> 3) * DM + red * 8 + (tid & 7);
    const float xv = __uint_as_float((u32)x[o] << 16);
    out[o] = __builtin_bit_cast(unsigned short, (__bf16)(xv + bf16r(bf16r(s) * gate)));
    if (red == 0 && tid == 0) *(u32*)(ws + WS_EPOCH) = tag;
    STAMPW(6, 0);
  }
}

extern "C" void kernel_launch(void* const* d_in, const int* in_sizes, int n_in, void* d_out, int out_size, void* d_ws,
                              size_t ws_size, hipStream_t stream) {
  (void)in_sizes; (void)n_in; (void)out_size;
  if (ws_size < WS_END || ws_size < WS_CNT + (size_t)NH * CNT_STRIDE) return;
  fused_decode_kernel<<<NWG, NTHR, 0, stream>>>((const unsigned short*)d_in[0], (const char*)d_in[1], (const char*)d_in[2],
                                                (const char*)d_in[3], (const char*)d_in[4], (const char*)d_in[6],
                                                (const char*)d_in[7], (const char*)d_in[8], (unsigned short*)d_out, (char*)d_ws);
}
```

```cpp
#include <hip/hip_runtime.h>
#include <stdint.h>

typedef unsigned u32;
typedef unsigned long long u64;
typedef u32 u32x4 __attribute__((ext_vector_type(4)));
typedef u32 u32x2 __attribute__((ext_vector_type(2)));
typedef float f32x4 __attribute__((ext_vector_type(4)));
typedef float f32x2 __attribute__((ext_vector_type(2)));
typedef __bf16 bf16x2 __attribute__((ext_vector_type(2)));

constexpr int DM = 512, NH = 32, SK = 16384, DH = 128, FF = 1376;
constexpr int NW = 8, NTHR = NW * 64, NWG = 256;
constexpr int UKEYS = 16;
constexpr int UPB = SK / UKEYS;
constexpr int UEND = 2 * UPB;
constexpr int NSTAT = 4;
#define NL1 80
constexpr int NLOC = UPB / 8 + NL1;
constexpr int GLOB = UPB - 8 * NL1;
constexpr int NSLICE = 43, FS = 32;
constexpr int NRED = 64;
constexpr u32 SPIN_CAP = 1u << 20;
#define LD_WQ ld_nt
#define HOLD 100
#define POLL_AT 8

constexpr size_t WS_EPOCH = 0;
#define CNT_OFF 8388608
#define CNT_STRIDE 256
constexpr size_t WS_CNT = CNT_OFF;
constexpr size_t WS_Q = 16384;
constexpr size_t WS_ML = WS_Q + 2 * NH * 64 * 8;
constexpr size_t WS_P = WS_ML + NH * 2 * 8 * 16;
constexpr size_t WS_T = WS_P + (size_t)NH * 8 * 2 * 8 * 128;
constexpr size_t WS_F = WS_T + (size_t)NRED * NWG * 128;
constexpr size_t WS_END = WS_F + (size_t)NRED * NSLICE * 128;

constexpr int L_XN = 0;
constexpr int L_REC = L_XN + 2 * DM;
constexpr int L_MM = L_REC + 2 * NW * DH;
constexpr int L_LL = L_MM + 16;
constexpr int L_O = L_LL + 16;
constexpr int L_HS = L_O + 32;
constexpr int L_CNT = L_HS + 64;
constexpr int L_FR = L_CNT + 4;
constexpr int L_BIG = L_FR + 512;
constexpr int L_TK = L_BIG + 4 * 2 * DM;
constexpr int L_END = L_TK + 4;

__device__ __forceinline__ float bf16r(float x) { return (float)(__bf16)x; }
__device__ __forceinline__ float lo16(u32 w) { return __uint_as_float(w << 16); }
__device__ __forceinline__ float hi16(u32 w) { return __uint_as_float(w & 0xffff0000u); }
__device__ __forceinline__ float dot2(u32 a, u32 b, float c) {
  return __builtin_amdgcn_fdot2_f32_bf16(__builtin_bit_cast(bf16x2, a), __builtin_bit_cast(bf16x2, b), c, false);
}
template <int CTRL> __device__ __forceinline__ float dpp(float x) {
  return __builtin_bit_cast(float, __builtin_amdgcn_mov_dpp(__builtin_bit_cast(int, x), CTRL, 0xf, 0xf, true));
}
constexpr int XOR1 = 0xB1, XOR2 = 0x4E, XOR7 = 0x141, XOR8 = 0x128, ROR4 = 0x124;
__device__ __forceinline__ float row_sum16(float x) {
  x += dpp<XOR1>(x); x += dpp<XOR2>(x); x += dpp<XOR7>(x); x += dpp<XOR8>(x); return x;
}
__device__ __forceinline__ float row_sum_q4(float x) {
  x += dpp<ROR4>(x); x += dpp<XOR8>(x); return x;
}
__device__ __forceinline__ float xrow_max(float x) {
  auto s = __builtin_amdgcn_permlane16_swap(__float_as_uint(x), __float_as_uint(x), false, false);
  x = fmaxf(__uint_as_float(s[0]), __uint_as_float(s[1]));
  auto t = __builtin_amdgcn_permlane32_swap(__float_as_uint(x), __float_as_uint(x), false, false);
  return fmaxf(__uint_as_float(t[0]), __uint_as_float(t[1]));
}
__device__ __forceinline__ float xrow_sum(float x) {
  auto s = __builtin_amdgcn_permlane16_swap(__float_as_uint(x), __float_as_uint(x), false, false);
  x = __uint_as_float(s[0]) + __uint_as_float(s[1]);
  auto t = __builtin_amdgcn_permlane32_swap(__float_as_uint(x), __float_as_uint(x), false, false);
  return __uint_as_float(t[0]) + __uint_as_float(t[1]);
}
__device__ __forceinline__ float wave_sum(float x) { return xrow_sum(row_sum16(x)); }

#define STAMPW(i, w) do {} while (0)
#define STAMPU(u) do {} while (0)

struct Stage { u32x4 k[4]; u32x4 v[4]; };

#define STAGE_REGS(s) "+v"(s.k[0]), "+v"(s.k[1]), "+v"(s.k[2]), "+v"(s.k[3]), "+v"(s.v[0]), "+v"(s.v[1]), "+v"(s.v[2]), "+v"(s.v[3])
__device__ __forceinline__ void ld_plain(u32x4& d, u32 voff, const char* base) {
  asm volatile("s_nop 4\n\tglobal_load_dwordx4 %0, %1, %2" : "=&v"(d) : "v"(voff), "s"(base) : "memory");
}
__device__ __forceinline__ void ld_nt2(u32x2& d, u32 voff, const char* base) {
  asm volatile("s_nop 4\n\tglobal_load_dwordx2 %0, %1, %2 nt" : "=&v"(d) : "v"(voff), "s"(base) : "memory");
}
__device__ __forceinline__ void ld_nt(u32x4& d, u32 voff, const char* base) {
  asm volatile("s_nop 4\n\tglobal_load_dwordx4 %0, %1, %2 nt" : "=&v"(d) : "v"(voff), "s"(base) : "memory");
}
__device__ __forceinline__ void ld_dev(u32x4& d, u32 voff, const char* base) {
  asm volatile("s_nop 4\n\tglobal_load_dwordx4 %0, %1, %2 sc1" : "=&v"(d) : "v"(voff), "s"(base) : "memory");
}
__device__ __forceinline__ void st_dev(u32 voff, char* base, float a, float b, u32 tag) {
  const u32x4 d = {__float_as_uint(a), tag, __float_as_uint(b), tag};
  asm volatile("s_nop 4\n\tglobal_store_dwordx4 %0, %1, %2 sc1" :: "v"(voff), "v"(d), "s"(base) : "memory");
}
__device__ __forceinline__ void ticket(u32& t, const char* cnt) {
  asm volatile("s_nop 4\n\ts_mov_b64 exec, 1\n\tglobal_atomic_add %0, %1, %2, %3 sc0\n\ts_mov_b64 exec, -1"
               : "=&v"(t) : "v"(0u), "v"(1u), "s"(cnt) : "memory");
}
template <int N> __device__ __forceinline__ u32 read_ticket(u32 t) {
  u32 v;
  asm volatile("s_waitcnt vmcnt(%2)\n\tv_readfirstlane_b32 %0, %1\n\ts_nop 4" : "=s"(v) : "v"(t), "i"(N) : "memory");
  return v;
}
__device__ __forceinline__ void issue(Stage& s, u32 voff, const char* kb, const char* vb) {
  asm volatile("s_nop 4\n\t"
               "global_load_dwordx4 %0, %8, %9 nt\n\t"
               "global_load_dwordx4 %1, %8, %9 offset:1024 nt\n\t"
               "global_load_dwordx4 %2, %8, %9 offset:2048 nt\n\t"
               "global_load_dwordx4 %3, %8, %9 offset:3072 nt\n\t"
               "global_load_dwordx4 %4, %8, %10 nt\n\t"
               "global_load_dwordx4 %5, %8, %10 offset:1024 nt\n\t"
               "global_load_dwordx4 %6, %8, %10 offset:2048 nt\n\t"
               "global_load_dwordx4 %7, %8, %10 offset:3072 nt"
               : "=&v"(s.k[0]), "=&v"(s.k[1]), "=&v"(s.k[2]), "=&v"(s.k[3]), "=&v"(s.v[0]), "=&v"(s.v[1]), "=&v"(s.v[2]), "=&v"(s.v[3])
               : "v"(voff), "s"(kb), "s"(vb) : "memory");
}
__device__ __forceinline__ void refill(Stage& s, u32& t, u32 voff, const char* kb, const char* vb, const char* cnt, int u) {
  asm volatile("s_nop 4\n\t"
               "s_cmpk_lt_i32 %15, 0x800\n\t"
               "s_cbranch_scc0 1f\n\t"
               "global_load_dwordx4 %0, %9, %10 nt\n\t"
               "global_load_dwordx4 %1, %9, %10 offset:1024 nt\n\t"
               "global_load_dwordx4 %2, %9, %10 offset:2048 nt\n\t"
               "global_load_dwordx4 %3, %9, %10 offset:3072 nt\n\t"
               "global_load_dwordx4 %4, %9, %11 nt\n\t"
               "global_load_dwordx4 %5, %9, %11 offset:1024 nt\n\t"
               "global_load_dwordx4 %6, %9, %11 offset:2048 nt\n\t"
               "global_load_dwordx4 %7, %9, %11 offset:3072 nt\n\t"
               "s_mov_b64 exec, 1\n\t"
               "global_atomic_add %8, %13, %14, %12 sc0\n\t"
               "s_mov_b64 exec, -1\n\t"
               "s_branch 2f\n"
               "1:\n\t"
               "s_waitcnt vmcnt(0)\n"
               "2:"
               : STAGE_REGS(s), "=&v"(t) : "v"(voff), "s"(kb), "s"(vb), "s"(cnt), "v"(0u), "v"(1u), "s"(u) : "memory", "scc");
}
__device__ __forceinline__ void refill_own(Stage& s, u32& t, u32 voff, const char* kb, const char* vb, const char* cnt, int g) {
  asm volatile("s_nop 4\n\t"
               "global_load_dwordx4 %0, %9, %10 nt\n\t"
               "global_load_dwordx4 %1, %9, %10 offset:1024 nt\n\t"
               "global_load_dwordx4 %2, %9, %10 offset:2048 nt\n\t"
               "global_load_dwordx4 %3, %9, %10 offset:3072 nt\n\t"
               "global_load_dwordx4 %4, %9, %11 nt\n\t"
               "global_load_dwordx4 %5, %9, %11 offset:1024 nt\n\t"
               "global_load_dwordx4 %6, %9, %11 offset:2048 nt\n\t"
               "global_load_dwordx4 %7, %9, %11 offset:3072 nt\n\t"
               "s_cmpk_lt_i32 %15, %16\n\t"
               "s_cbranch_scc1 1f\n\t"
               "s_mov_b64 exec, 1\n\t"
               "global_atomic_add %8, %13, %14, %12 sc0\n\t"
               "s_mov_b64 exec, -1\n"
               "1:"
               : STAGE_REGS(s), "+v"(t) : "v"(voff), "s"(kb), "s"(vb), "s"(cnt), "v"(0u), "v"(1u), "s"(g), "i"(NLOC - 2 * NW) : "memory", "scc");
}
static_assert(UEND == 0x800, "refill compares against 0x800");
template <int N> __device__ __forceinline__ void wait_k(Stage& s) {
  asm volatile("s_waitcnt vmcnt(%4)" : "+v"(s.k[0]), "+v"(s.k[1]), "+v"(s.k[2]), "+v"(s.k[3]) : "i"(N) : "memory");
}
template <int N> __device__ __forceinline__ void wait_v(Stage& s) {
  asm volatile("s_waitcnt vmcnt(%4)" : "+v"(s.v[0]), "+v"(s.v[1]), "+v"(s.v[2]), "+v"(s.v[3]) : "i"(N) : "memory");
}
template <int N, typename V> __device__ __forceinline__ void wait_1(V& d) { asm volatile("s_waitcnt vmcnt(%1)" : "+v"(d) : "i"(N) : "memory"); }
template <int N> __device__ __forceinline__ void wait_n() { asm volatile("s_waitcnt vmcnt(%0)" :: "i"(N) : "memory"); }

template <int LATER>
__device__ __forceinline__ void consume(Stage& s, const u32x4 q, float& m, float& l, float (&acc)[8]) {
  constexpr float SCALE = 0.08838834764831845f, LOG2E = 1.4426950408889634f;
  float y[4];
  wait_k<LATER + 4>(s);
#pragma unroll
  for (int t = 0; t < 4; ++t) {
    float d = dot2(s.k[t][0], q[0], 0.f);
    d = dot2(s.k[t][1], q[1], d); d = dot2(s.k[t][2], q[2], d); d = dot2(s.k[t][3], q[3], d);
    d = row_sum16(d);
    y[t] = bf16r(bf16r(d) * SCALE) * LOG2E;
  }
  float mn = m;
#pragma unroll
  for (int t = 0; t < 4; ++t) mn = fmaxf(mn, y[t]);
  const float alpha = __builtin_amdgcn_exp2f(m - mn);
  float ps = 0.f;
#pragma unroll
  for (int e = 0; e < 8; ++e) acc[e] *= alpha;
  wait_v<LATER>(s);
#pragma unroll
  for (int t = 0; t < 4; ++t) {
    const float p = __builtin_amdgcn_exp2f(y[t] - mn);
    ps += p;
#pragma unroll
    for (int e = 0; e < 4; ++e) {
      acc[2 * e] = fmaf(p, lo16(s.v[t][e]), acc[2 * e]);
      acc[2 * e + 1] = fmaf(p, hi16(s.v[t][e]), acc[2 * e + 1]);
    }
  }
  l = fmaf(l, alpha, ps);
  m = mn;
  asm volatile("" : "+v"(acc[0]), "+v"(acc[1]), "+v"(acc[2]), "+v"(acc[3]), "+v"(acc[4]), "+v"(acc[5]), "+v"(acc[6]), "+v"(acc[7]), "+v"(l), "+v"(m));
  __builtin_amdgcn_sched_barrier(0);
}

constexpr float M_NONE = -1.0e30f;

__device__ __forceinline__ u32 lds_inc(float* lds, int idx) {
  return __hip_atomic_fetch_add(&reinterpret_cast<u32*>(lds)[idx], 1u, __ATOMIC_RELAXED, __HIP_MEMORY_SCOPE_WORKGROUP);
}

__device__ __forceinline__ void layer_norm(const u32x4 (&xr)[2], float* lds, int lane) {
#pragma unroll
  for (int bb = 0; bb < 2; ++bb) {
    float xv[8];
#pragma unroll
    for (int e = 0; e < 4; ++e) { xv[2 * e] = lo16(xr[bb][e]); xv[2 * e + 1] = hi16(xr[bb][e]); }
    float s = 0.f;
#pragma unroll
    for (int e = 0; e < 8; ++e) s += xv[e];
    const float mean = wave_sum(s) * (1.f / DM);
    float s2 = 0.f;
#pragma unroll
    for (int e = 0; e < 8; ++e) { const float d = xv[e] - mean; s2 = fmaf(d, d, s2); }
    const float var = bf16r(wave_sum(s2) * (1.f / DM));
    const float mb = bf16r(mean);
    const float den = bf16r(sqrtf(bf16r(var + 1e-5f)));
    f32x4 o0, o1;
#pragma unroll
    for (int e = 0; e < 4; ++e) { o0[e] = bf16r(bf16r(xv[e] - mb) / den); o1[e] = bf16r(bf16r(xv[4 + e] - mb) / den); }
    *reinterpret_cast<f32x4*>(&lds[L_XN + bb * DM + lane * 8]) = o0;
    *reinterpret_cast<f32x4*>(&lds[L_XN + bb * DM + lane * 8 + 4]) = o1;
  }
  asm volatile("s_waitcnt lgkmcnt(0)" ::: "memory");
}

__global__ void __launch_bounds__(NTHR)
fused_decode_kernel(const unsigned short* __restrict__ x, const char* __restrict__ kc, const char* __restrict__ vc,
                    const char* __restrict__ wq, const char* __restrict__ wo, const char* __restrict__ wg,
                    const char* __restrict__ wu, const char* __restrict__ wd, unsigned short* __restrict__ out,
                    char* __restrict__ ws) {
  __shared__ __attribute__((aligned(16))) float lds[L_END];
  const int tid = threadIdx.x, lane = tid & 63, wave = __builtin_amdgcn_readfirstlane(tid >> 6);
  const int r = lane >> 4, c = lane & 15;
  const int id = blockIdx.x, i = id & 7, h = id >> 3;
  int slice = -1;
  if (i == ((h >> 1) & 7)) slice = h;
  else if (h % 3 == 0 && i == (((h / 6) * 3 + 5) & 7)) slice = 32 + h / 3;
  const int dd = ((h >> 1) - i) & 7;
  const bool is_q = (dd & 3) == 2;
  const bool ffn_wave = slice >= 0 && wave >= 1 && wave <= 4;
  const bool is_red = (i & 3) == (h & 3);
  const int red = 2 * h + (i >> 2);
  const u32 voff = lane * 16;
  const u32 tag = *(const u32*)(ws + WS_EPOCH) + 1u;
  const u32 ctag = (0x3C5A7u - tag) & 0xfffffu;
  const char* cnt = ws + WS_CNT + (size_t)h * CNT_STRIDE;
  STAMPW(0, 0);
  if (tid < 4) reinterpret_cast<u32*>(lds)[L_CNT + tid] = 0u;
  if (tid == 4) reinterpret_cast<u32*>(lds)[L_TK] = (u32)(NW * (NSTAT + 2));
  __syncthreads();

  if (i == 0 && tid == 0)
    __hip_atomic_store((__attribute__((address_space(1))) u32*)(ws + WS_CNT + (size_t)h * CNT_STRIDE), ctag << 12,
                       __ATOMIC_RELAXED, __HIP_MEMORY_SCOPE_AGENT);
  u32x2 wor[4];
  auto load_wo = [&]() {
#pragma unroll
    for (int k = 0; k < 4; ++k) ld_nt2(wor[k], (u32)((4 * r + k) * (DM * 2) + c * 8), wo + (size_t)(h * DH + 16 * i) * (DM * 2) + wave * 128);
  };
  load_wo();
  if (!is_q && !ffn_wave) {
    const u64 t_start = __builtin_amdgcn_s_memrealtime();
    const size_t hb = (size_t)h * SK * DH * 2 + (size_t)(i * (UPB / 8) + wave) * (UKEYS * DH * 2);
    u32 t0, t1, t2;
    asm volatile("global_load_dword %0, %3, %4 nt\n\tglobal_load_dword %1, %3, %5 nt\n\tglobal_load_dword %2, %3, %6 sc1"
                 : "=&v"(t0), "=&v"(t1), "=&v"(t2) : "v"(0u), "s"(kc + hb), "s"(vc + hb), "s"(ws + WS_Q + h * 512) : "memory");
    for (int spins = 0; spins < 4096 && __builtin_amdgcn_s_memrealtime() - t_start < HOLD; ++spins) __builtin_amdgcn_s_sleep(2);
    asm volatile("s_waitcnt vmcnt(0)" : "+v"(t0), "+v"(t1), "+v"(t2) :: "memory");
  }
  if (is_q) {
    const int qi = 2 * h + (dd >> 2);
    u32x4 xr[2], wr[8];
    ld_plain(xr[0], voff, (const char*)x);
    ld_plain(xr[1], voff, (const char*)x + 1024);
    {
      const u32 vo = (lane >> 3) * (NH * DH * 2) + (lane & 7) * 16;
#pragma unroll
      for (int k = 0; k < 8; ++k) LD_WQ(wr[k], vo, wq + qi * 128 + (size_t)(64 * wave + 8 * k) * (NH * DH * 2));
    }
    wait_1<8>(xr[0]); wait_1<8>(xr[1]);
    layer_norm(xr, lds, lane);
    float qa[2][8];
#pragma unroll
    for (int e = 0; e < 8; ++e) { qa[0][e] = 0.f; qa[1][e] = 0.f; }
    for (int z = 0; z < 8; ++z) wait_1<0>(wr[z]);
#pragma unroll
    for (int k = 0; k < 8; ++k) {
      const float x0 = lds[L_XN + 64 * wave + (lane >> 3) + 8 * k], x1 = lds[L_XN + DM + 64 * wave + (lane >> 3) + 8 * k];
#pragma unroll
      for (int e = 0; e < 4; ++e) {
        const float a = lo16(wr[k][e]), bq = hi16(wr[k][e]);
        qa[0][2 * e] = fmaf(x0, a, qa[0][2 * e]); qa[0][2 * e + 1] = fmaf(x0, bq, qa[0][2 * e + 1]);
        qa[1][2 * e] = fmaf(x1, a, qa[1][2 * e]); qa[1][2 * e + 1] = fmaf(x1, bq, qa[1][2 * e + 1]);
      }
    }
#pragma unroll
    for (int bb = 0; bb < 2; ++bb) {
#pragma unroll
      for (int e = 0; e < 8; ++e) { const float t = qa[bb][e] + dpp<XOR8>(qa[bb][e]); qa[bb][e] = xrow_sum(t); }
      if (lane < 8) {
        float* rec = &lds[L_BIG + (wave * 2 + bb) * 64 + lane * 8];
        *reinterpret_cast<f32x4*>(rec) = f32x4{qa[bb][0], qa[bb][1], qa[bb][2], qa[bb][3]};
        *reinterpret_cast<f32x4*>(rec + 4) = f32x4{qa[bb][4], qa[bb][5], qa[bb][6], qa[bb][7]};
      }
    }
    asm volatile("s_waitcnt lgkmcnt(0)" ::: "memory");
    u32 arr = 0;
    if (lane == 0) arr = lds_inc(lds, L_CNT + 3);
    if (__builtin_amdgcn_readfirstlane(arr) == NW - 1) {
      asm volatile("" ::: "memory");
      if (lane < 32) {
        f32x4 t = {0.f, 0.f, 0.f, 0.f};
#pragma unroll
        for (int w = 0; w < NW; ++w) t += *reinterpret_cast<const f32x4*>(&lds[L_BIG + w * 128 + lane * 4]);
        const bf16x2 p0 = {(__bf16)t[0], (__bf16)t[1]}, p1 = {(__bf16)t[2], (__bf16)t[3]};
        st_dev((u32)(((lane >> 4) * NH * 64 + qi * 32 + (lane & 15) * 2) * 8), ws + WS_Q,
               __builtin_bit_cast(float, p0), __builtin_bit_cast(float, p1), tag);
      }
      STAMPW(7, wave);
    }
  } else if (ffn_wave) {
    const int fw = wave - 1, ft = tid - 64;
    u32x4 xr[2], gw[8], uw[8], dw[8];
    ld_plain(xr[0], voff, (const char*)x);
    ld_plain(xr[1], voff, (const char*)x + 1024);
    {
      const u32 vo = (u32)((ft >> 2) * (FF * 2) + (ft & 3) * 16);
#pragma unroll
      for (int k = 0; k < 8; ++k) ld_nt(gw[k], vo, wg + slice * (FS * 2) + (size_t)k * 64 * (FF * 2));
#pragma unroll
      for (int k = 0; k < 8; ++k) ld_nt(uw[k], vo, wu + slice * (FS * 2) + (size_t)k * 64 * (FF * 2));
#pragma unroll
      for (int k = 0; k < 8; ++k) ld_nt(dw[k], voff, wd + (size_t)(slice * FS + 8 * fw + k) * (DM * 2));
    }
    wait_1<24>(xr[0]); wait_1<24>(xr[1]);
    layer_norm(xr, lds, lane);
    for (int z = 0; z < 8; ++z) { wait_1<8>(gw[z]); wait_1<8>(uw[z]); }
#pragma unroll
    for (int bb = 0; bb < 2; ++bb) {
      float ga[8], ua[8];
#pragma unroll
      for (int e = 0; e < 8; ++e) { ga[e] = 0.f; ua[e] = 0.f; }
#pragma unroll
      for (int k = 0; k < 8; ++k) {
        const float xv = lds[L_XN + bb * DM + (ft >> 2) + 64 * k];
#pragma unroll
        for (int e = 0; e < 4; ++e) {
          ga[2 * e] = fmaf(xv, lo16(gw[k][e]), ga[2 * e]); ga[2 * e + 1] = fmaf(xv, hi16(gw[k][e]), ga[2 * e + 1]);
          ua[2 * e] = fmaf(xv, lo16(uw[k][e]), ua[2 * e]); ua[2 * e + 1] = fmaf(xv, hi16(uw[k][e]), ua[2 * e + 1]);
        }
      }
#pragma unroll
      for (int e = 0; e < 8; ++e) { ga[e] = xrow_sum(row_sum_q4(ga[e])); ua[e] = xrow_sum(row_sum_q4(ua[e])); }
      if (lane < 4) {
        float* rec = &lds[L_FR + fw * 128 + bb * 64 + lane * 8];
        *reinterpret_cast<f32x4*>(rec) = f32x4{ga[0], ga[1], ga[2], ga[3]};
        *reinterpret_cast<f32x4*>(rec + 4) = f32x4{ga[4], ga[5], ga[6], ga[7]};
        *reinterpret_cast<f32x4*>(rec + 32) = f32x4{ua[0], ua[1], ua[2], ua[3]};
        *reinterpret_cast<f32x4*>(rec + 36) = f32x4{ua[4], ua[5], ua[6], ua[7]};
      }
    }
    asm volatile("s_waitcnt lgkmcnt(0)" ::: "memory");
    u32 arr = 0;
    if (lane == 0) arr = lds_inc(lds, L_CNT + 0);
    if (__builtin_amdgcn_readfirstlane(arr) == 3) {
      asm volatile("" ::: "memory");
      const int bb = lane >> 5, col = lane & 31;
      float g = 0.f, u = 0.f;
#pragma unroll
      for (int w = 0; w < 4; ++w) { g += lds[L_FR + w * 128 + bb * 64 + col]; u += lds[L_FR + w * 128 + bb * 64 + 32 + col]; }
      g = bf16r(g); u = bf16r(u);
      const float inner = 0.7978845608028654f * (g + 0.044715f * g * g * g);
      const float ge = bf16r(0.5f * g * (1.f + tanhf(inner)));
      lds[L_HS + lane] = bf16r(ge * u);
      asm volatile("s_waitcnt lgkmcnt(0)" ::: "memory");
      if (lane == 0) __hip_atomic_store(&reinterpret_cast<u32*>(lds)[L_CNT + 1], 1u, __ATOMIC_RELAXED, __HIP_MEMORY_SCOPE_WORKGROUP);
    }
    for (u32 spins = 0; spins < SPIN_CAP; ++spins) {
      if (__hip_atomic_load(&reinterpret_cast<u32*>(lds)[L_CNT + 1], __ATOMIC_RELAXED, __HIP_MEMORY_SCOPE_WORKGROUP) != 0u) break;
      __builtin_amdgcn_s_sleep(1);
    }
    asm volatile("" ::: "memory");
    for (int z = 0; z < 8; ++z) wait_1<0>(dw[z]);
#pragma unroll
    for (int bb = 0; bb < 2; ++bb) {
      float da[8];
#pragma unroll
      for (int e = 0; e < 8; ++e) da[e] = 0.f;
#pragma unroll
      for (int k = 0; k < 8; ++k) {
        const float hv = lds[L_HS + bb * 32 + 8 * fw + k];
#pragma unroll
        for (int e = 0; e < 4; ++e) { da[2 * e] = fmaf(hv, lo16(dw[k][e]), da[2 * e]); da[2 * e + 1] = fmaf(hv, hi16(dw[k][e]), da[2 * e + 1]); }
      }
      float* rec = &lds[L_BIG + (fw * 2 + bb) * DM + lane * 8];
      *reinterpret_cast<f32x4*>(rec) = f32x4{da[0], da[1], da[2], da[3]};
      *reinterpret_cast<f32x4*>(rec + 4) = f32x4{da[4], da[5], da[6], da[7]};
    }
    asm volatile("s_waitcnt lgkmcnt(0)" ::: "memory");
    if (lane == 0) arr = lds_inc(lds, L_CNT + 2);
    if (__builtin_amdgcn_readfirstlane(arr) == 3) {
      asm volatile("" ::: "memory");
      const int pc = lane & 7;
#pragma unroll
      for (int k = 0; k < 8; ++k) {
        const int rd = 8 * k + (lane >> 3);
        const int idx = (pc >> 2) * DM + rd * 8 + 2 * (pc & 3);
        float s0 = 0.f, s1 = 0.f;
#pragma unroll
        for (int w = 0; w < 4; ++w) { const f32x2 t2 = *reinterpret_cast<const f32x2*>(&lds[L_BIG + w * 2 * DM + idx]); s0 += t2[0]; s1 += t2[1]; }
        st_dev((u32)((rd * NSLICE + slice) * 128 + pc * 16), ws + WS_F, s0, s1, tag);
      }
    }
    STAMPW(2, 1);
  }

  constexpr size_t UB = (size_t)UKEYS * DH * 2;
  const char* kp = kc + (size_t)h * SK * DH * 2;
  const char* vp = vc + (size_t)h * SK * DH * 2;
  auto uoff = [&](int u) -> size_t {
    u = min(u, UEND - 1);
    return (size_t)(u & (UPB - 1)) * UB + (size_t)(u >> 10) * ((size_t)NH * SK * DH * 2);
  };
  Stage A, B, C, D;
  const int us = i * (UPB / 8) + wave;
  issue(A, voff, kp + us * UB, vp + us * UB);
  issue(B, voff, kp + (NW + us) * UB, vp + (NW + us) * UB);
  issue(C, voff, kp + (2 * NW + us) * UB, vp + (2 * NW + us) * UB);
  STAMPW(24 + wave, wave);
  wait_n<POLL_AT>();
  u32 tA = 0, tB = 0;
  u32x4 q0, q1;
  {
    u32x4 g[4];
    const u32 qo = (u32)((h * 64 + 4 * c) * 8);
    ld_dev(g[0], qo, ws + WS_Q); ld_dev(g[1], qo + 16, ws + WS_Q);
    ld_dev(g[2], qo, ws + WS_Q + NH * 64 * 8); ld_dev(g[3], qo + 16, ws + WS_Q + NH * 64 * 8);
    issue(D, voff, kp + (3 * NW + us) * UB, vp + (3 * NW + us) * UB);
    wait_1<8>(g[0]); wait_1<8>(g[1]); wait_1<8>(g[2]); wait_1<8>(g[3]);
    for (u32 spins = 0; spins < SPIN_CAP; ++spins) {
      bool ok = true;
#pragma unroll
      for (int k = 0; k < 4; ++k) ok = ok && g[k][1] == tag && g[k][3] == tag;
      if (__all(ok)) break;
      __builtin_amdgcn_s_sleep(2);
      ld_dev(g[0], qo, ws + WS_Q); ld_dev(g[1], qo + 16, ws + WS_Q);
      ld_dev(g[2], qo, ws + WS_Q + NH * 64 * 8); ld_dev(g[3], qo + 16, ws + WS_Q + NH * 64 * 8);
      wait_1<0>(g[0]); wait_1<0>(g[1]); wait_1<0>(g[2]); wait_1<0>(g[3]);
    }
    q0 = u32x4{g[0][0], g[0][2], g[1][0], g[1][2]};
    q1 = u32x4{g[2][0], g[2][2], g[3][0], g[3][2]};
  }
  STAMPW(8 + wave, wave);
  float m = M_NONE, l = 0.f, acc[8];
#pragma unroll
  for (int e = 0; e < 8; ++e) acc[e] = 0.f;
  auto next_unit = [&](u32 val) -> int {
    for (u32 spins = 0; (val >> 12) != ctag; ++spins) {
      if (spins >= SPIN_CAP) return UEND;
      u32 t2;
      ticket(t2, cnt); val = read_ticket<0>(t2);
    }
    return (val & 0xfffu) < (u32)GLOB ? UPB + (int)(val & 0xfffu) : UEND;
  };
  auto flush = [&](int bb) {
    const float mw = xrow_max(m);
    const float sc = __builtin_amdgcn_exp2f(m - mw);
    const float lw = xrow_sum(l * sc);
    float a[8];
#pragma unroll
    for (int e = 0; e < 8; ++e) a[e] = xrow_sum(acc[e] * sc);
    if (r == 0) {
      float* rec = &lds[L_REC + (bb * NW + wave) * DH + 8 * c];
      *reinterpret_cast<f32x4*>(rec) = f32x4{a[0], a[1], a[2], a[3]};
      *reinterpret_cast<f32x4*>(rec + 4) = f32x4{a[4], a[5], a[6], a[7]};
      if (c == 0) { lds[L_MM + bb * NW + wave] = mw; lds[L_LL + bb * NW + wave] = lw; }
    }
    m = M_NONE; l = 0.f;
#pragma unroll
    for (int e = 0; e < 8; ++e) acc[e] = 0.f;
  };
  int uA, uB;
  for (int k = 0; k < 4; ++k) wait_1<8>(wor[k]);
  auto draw = [&]() -> u32 { u32 n = 0; if (lane == 0) n = lds_inc(lds, L_TK); return n; };
  auto local_unit = [&](int n) -> int { return n < UPB / 8 ? i * (UPB / 8) + n : UPB + GLOB + NL1 * i + (n - UPB / 8); };
  uA = us + 4 * NW; uB = us + 5 * NW;
  consume<8>(A, q0, m, l, acc); issue(A, voff, kp + uA * UB, vp + uA * UB);
  consume<8>(B, q0, m, l, acc); issue(B, voff, kp + uB * UB, vp + uB * UB);
  consume<16>(C, q0, m, l, acc);
  consume<16>(D, q0, m, l, acc);
  u32x4 q = q0;
  int curb = 0;
  static_assert(NLOC - 2 * NW >= NW * (NSTAT + 2), "window");
  bool gB = false, skipA = false;
  for (;;) {
    const u32 dA = draw();
    if (uA >= UPB && curb == 0) { flush(0); q = q1; curb = 1; }
    consume<8>(A, q, m, l, acc);
    const int nA = __builtin_amdgcn_readfirstlane(dA);
    uA = local_unit(nA);
    const bool gA = nA >= NLOC - 2 * NW;
    refill_own(A, tA, voff, kp + uoff(uA), vp + uoff(uA), cnt, nA);
    if (gB) { skipA = true; break; }
    const u32 dB = draw();
    if (uB >= UPB && curb == 0) { flush(0); q = q1; curb = 1; }
    consume<8>(B, q, m, l, acc);
    const int nB = __builtin_amdgcn_readfirstlane(dB);
    uB = local_unit(nB);
    gB = nB >= NLOC - 2 * NW;
    refill_own(B, tB, voff, kp + uoff(uB), vp + uoff(uB), cnt, nB);
    if (gA) break;
  }
  int fails = (uA >= UEND ? 1 : 0) + (uB >= UEND ? 1 : 0);
  while (fails < 2) {
    if (uA < UEND && !skipA) {
      if (fails) wait_n<0>();
      if (uA >= UPB && curb == 0) { flush(0); q = q1; curb = 1; }
      consume<10>(A, q, m, l, acc);
      uA = next_unit(read_ticket<9>(tA)); refill(A, tA, voff, kp + uoff(uA), vp + uoff(uA), cnt, uA);
      STAMPU(uA);
      if (uA >= UEND) ++fails;
    }
    skipA = false;
    if (uB < UEND) {
      if (fails) wait_n<0>();
      if (uB >= UPB && curb == 0) { flush(0); q = q1; curb = 1; }
      consume<10>(B, q, m, l, acc);
      uB = next_unit(read_ticket<9>(tB)); refill(B, tB, voff, kp + uoff(uB), vp + uoff(uB), cnt, uB);
      STAMPU(uB);
      if (uB >= UEND) ++fails;
    }
  }
  STAMPW(16 + wave, wave);
  if (curb == 0) flush(0);
  flush(1);
  __syncthreads();

  if (wave < 2) {
    const int bb = 1 - wave;
    const f32x4 m0 = *reinterpret_cast<const f32x4*>(&lds[L_MM + bb * NW]), m1 = *reinterpret_cast<const f32x4*>(&lds[L_MM + bb * NW + 4]);
    const f32x4 l0 = *reinterpret_cast<const f32x4*>(&lds[L_LL + bb * NW]), l1 = *reinterpret_cast<const f32x4*>(&lds[L_LL + bb * NW + 4]);
    const float M = fmaxf(fmaxf(fmaxf(m0[0], m0[1]), fmaxf(m0[2], m0[3])), fmaxf(fmaxf(m1[0], m1[1]), fmaxf(m1[2], m1[3])));
    float L = 0.f, a0 = 0.f, a1 = 0.f;
#pragma unroll
    for (int w = 0; w < NW; ++w) {
      const float wi = __builtin_amdgcn_exp2f((w < 4 ? m0[w & 3] : m1[w & 3]) - M);
      L = fmaf(wi, w < 4 ? l0[w & 3] : l1[w & 3], L);
      const f32x2 a2 = *reinterpret_cast<const f32x2*>(&lds[L_REC + (bb * NW + w) * DH + 2 * lane]);
      a0 = fmaf(wi, a2[0], a0); a1 = fmaf(wi, a2[1], a1);
    }
    st_dev((u32)(((((lane >> 3) * 2 + bb) * 8 + i) * 16 + 2 * (lane & 7)) * 8), ws + WS_P + (size_t)h * (8 * 2 * 8 * 128), a0, a1, tag);
    if (lane == 0) st_dev((u32)(((h * 2 + bb) * 8 + i) * 16), ws + WS_ML, M, L, tag);
  }
  STAMPW(4, 0);
  if (wave == 0) {
    const int bb = (lane >> 3) & 1, j = lane & 7;
    const u32 po = (u32)(((bb * 8 + j) * 16 + 4 * r) * 8), mo = (u32)(((h * 2 + bb) * 8 + j) * 16);
    const char* pb = ws + WS_P + (size_t)(h * 8 + i) * (2 * 8 * 128);
    u32x4 g0, g1, gm;
    for (u32 spins = 0; spins < SPIN_CAP; ++spins) {
      ld_dev(g0, po, pb); ld_dev(g1, po + 16, pb); ld_dev(gm, mo, ws + WS_ML);
      wait_1<0>(g0); wait_1<0>(g1); wait_1<0>(gm);
      const bool ok = g0[1] == tag && g0[3] == tag && g1[1] == tag && g1[3] == tag && gm[1] == tag && gm[3] == tag;
      if (__all(ok)) break;
      __builtin_amdgcn_s_sleep(1);
    }
    const float mj = __uint_as_float(gm[0]), lj = __uint_as_float(gm[2]);
    float Mh = fmaxf(mj, dpp<XOR1>(mj)); Mh = fmaxf(Mh, dpp<XOR2>(Mh)); Mh = fmaxf(Mh, dpp<XOR7>(Mh));
    const float wj = __builtin_amdgcn_exp2f(mj - Mh);
    float v[5] = {wj * lj, wj * __uint_as_float(g0[0]), wj * __uint_as_float(g0[2]), wj * __uint_as_float(g1[0]), wj * __uint_as_float(g1[2])};
#pragma unroll
    for (int e = 0; e < 5; ++e) { v[e] += dpp<XOR1>(v[e]); v[e] += dpp<XOR2>(v[e]); v[e] += dpp<XOR7>(v[e]); }
    if (j == 0) *reinterpret_cast<f32x4*>(&lds[L_O + bb * 16 + 4 * r]) = f32x4{bf16r(v[1] / v[0]), bf16r(v[2] / v[0]), bf16r(v[3] / v[0]), bf16r(v[4] / v[0])};
  } else if (is_red && wave >= 2) {
    const int e = tid - 128;
    if (e < NSLICE * 8) {
      u32x4 g;
      for (u32 spins = 0; spins < SPIN_CAP; ++spins) {
        ld_dev(g, (u32)(e * 16), ws + WS_F + (size_t)red * (NSLICE * 128));
        wait_1<0>(g);
        if (__all(g[1] == tag && g[3] == tag)) break;
        __builtin_amdgcn_s_sleep(4);
      }
      *reinterpret_cast<f32x2*>(&lds[L_BIG + 2 * e]) = f32x2{__uint_as_float(g[0]), __uint_as_float(g[2])};
    }
  }
  __syncthreads();
  STAMPW(5, 0);
  {
    const f32x4 o0 = *reinterpret_cast<const f32x4*>(&lds[L_O + 4 * r]), o1 = *reinterpret_cast<const f32x4*>(&lds[L_O + 16 + 4 * r]);
    float t0[4], t1[4];
#pragma unroll
    for (int e = 0; e < 4; ++e) { t0[e] = 0.f; t1[e] = 0.f; }
#pragma unroll
    for (int k = 0; k < 4; ++k) {
      const float w4[4] = {lo16(wor[k][0]), hi16(wor[k][0]), lo16(wor[k][1]), hi16(wor[k][1])};
#pragma unroll
      for (int e = 0; e < 4; ++e) { t0[e] = fmaf(o0[k], w4[e], t0[e]); t1[e] = fmaf(o1[k], w4[e], t1[e]); }
    }
#pragma unroll
    for (int e = 0; e < 4; ++e) { t0[e] = xrow_sum(t0[e]); t1[e] = xrow_sum(t1[e]); }
    const float sa = r == 0 ? t0[0] : r == 1 ? t0[2] : r == 2 ? t1[0] : t1[2];
    const float sb = r == 0 ? t0[1] : r == 1 ? t0[3] : r == 2 ? t1[1] : t1[3];
    st_dev((u32)(((8 * wave + (c >> 1)) * NWG + id) * 128 + (r >> 1) * 64 + ((c & 1) * 4 + (r & 1) * 2) * 8), ws + WS_T, sa, sb, tag);
  }
  if (!is_red) return;

  float gate = 0.f;
  if (tid < 16) {
    float d = 0.f;
#pragma unroll
    for (int s = 0; s < NSLICE; ++s) d += lds[L_BIG + s * 16 + tid];
    d = bf16r(d);
    gate = bf16r(d / (1.f + __expf(-d)));
  }
  {
    const char* tb = ws + WS_T + (size_t)red * (NWG * 128);
    u32x4 g[4];
    for (u32 spins = 0; spins < SPIN_CAP; ++spins) {
#pragma unroll
      for (int k = 0; k < 4; ++k) ld_dev(g[k], (u32)(tid * 16), tb + k * 8192);
      bool ok = true;
#pragma unroll
      for (int k = 0; k < 4; ++k) { wait_1<0>(g[k]); ok = ok && g[k][1] == tag && g[k][3] == tag; }
      if (__all(ok)) break;
      __builtin_amdgcn_s_sleep(1);
    }
    float s0 = 0.f, s1 = 0.f;
#pragma unroll
    for (int k = 0; k < 4; ++k) { s0 += __uint_as_float(g[k][0]); s1 += __uint_as_float(g[k][2]); }
    s0 += dpp<XOR8>(s0); s1 += dpp<XOR8>(s1);
    s0 = xrow_sum(s0); s1 = xrow_sum(s1);
    if (lane < 8) *reinterpret_cast<f32x2*>(&lds[L_FR + wave * 16 + 2 * lane]) = f32x2{s0, s1};
  }
  __syncthreads();
  if (tid < 16) {
    float s = 0.f;
#pragma unroll
    for (int w = 0; w < NW; ++w) s += lds[L_FR + w * 16 + tid];
    const int o = (tid >> 3) * DM + red * 8 + (tid & 7);
    const float xv = __uint_as_float((u32)x[o] << 16);
    out[o] = __builtin_bit_cast(unsigned short, (__bf16)(xv + bf16r(bf16r(s) * gate)));
    if (red == 0 && tid == 0) *(u32*)(ws + WS_EPOCH) = tag;
    STAMPW(6, 0);
  }
}

extern "C" void kernel_launch(void* const* d_in, const int* in_sizes, int n_in, void* d_out, int out_size, void* d_ws,
                              size_t ws_size, hipStream_t stream) {
  (void)in_sizes; (void)n_in; (void)out_size;
  if (ws_size < WS_END || ws_size < WS_CNT + (size_t)NH * CNT_STRIDE) return;
  fused_decode_kernel<<<NWG, NTHR, 0, stream>>>((const unsigned short*)d_in[0], (const char*)d_in[1], (const char*)d_in[2],
                                                (const char*)d_in[3], (const char*)d_in[4], (const char*)d_in[6],
                                                (const char*)d_in[7], (const char*)d_in[8], (unsigned short*)d_out, (char*)d_ws);
}
```
